```python
import torch

def _softmax(x, axis=-1):
    return torch.softmax(x, dim=axis)
def _gelu(x):
    return torch.nn.functional.gelu(x, approximate="tanh")
def _silu(x):
    return torch.nn.functional.silu(x)
def _sigmoid(x):
    return torch.sigmoid(x)
def _layernorm(x, axis=-1, eps=1e-5):
    m = x.mean(dim=axis, keepdim=True)
    v = x.var(dim=axis, keepdim=True, unbiased=False)
    return (x - m) / torch.sqrt(v + eps)
def _scatter_add(tgt, idx, src, axis=0):
    return tgt.clone().index_add_(axis, idx, src)
def _segment_sum(src, seg, n):
    out = torch.zeros((n,) + src.shape[1:], dtype=src.dtype)
    return out.index_add_(0, seg, src)
def _pad(x, pw):
    flat = tuple(v for pair in reversed(pw) for v in pair)
    return torch.nn.functional.pad(x, flat)

kernel_name = 'sequential_preLN__ssm_conv_s4d__dense_activation_squared_relu__prompt'

def setup_inputs():

    _w = (lambda a: (lambda t: t.to(torch.bfloat16) if t.is_floating_point() else t)(torch.as_tensor(a)))
    def _A_setup():
        Bsz, L, D, N = 1, 2048, 576, 128
        return {
            "u": _w((lambda *s: torch.randn(*s, dtype=torch.bfloat16) * 0.05)(Bsz, L, D)),
            "log_A_real": _w((lambda *s: torch.randn(*s, dtype=torch.bfloat16) * 3.2)(D, N) * 0.5),
            "A_imag": _w((lambda *s: torch.randn(*s, dtype=torch.bfloat16) * 0.05)(D, N) * 3.14),
            "C_real": _w((lambda *s: torch.randn(*s, dtype=torch.bfloat16) * 0.1)(D, N) / N ** 0.5),
            "C_imag": _w((lambda *s: torch.randn(*s, dtype=torch.bfloat16) * 0.1)(D, N) / N ** 0.5),
            "log_dt": _w((lambda *s: torch.randn(*s, dtype=torch.bfloat16) * 0.05)(D) * 0.5 - 4.0),
            "D_skip": _w((lambda *s: torch.randn(*s, dtype=torch.bfloat16) * 0.05)(D)),
        }
    _A_in = _A_setup()

    def _B_setup():
        B, S, D, F = 1, 2048, 576, 4608
        return {
            "x": _w((lambda *s: torch.randn(*s, dtype=torch.bfloat16) * 0.05)(B, S, D)),
            "w1": _w((lambda *s: torch.randn(*s, dtype=torch.bfloat16) * 0.05)(D, F)),
            "b1": _w((lambda *s: torch.randn(*s, dtype=torch.bfloat16) * 0.2)(F)),
            "w2": _w((lambda *s: torch.randn(*s, dtype=torch.bfloat16) * 0.05)(F, D)),
            "b2": _w((lambda *s: torch.randn(*s, dtype=torch.bfloat16) * 1.6)(D)),
        }
    _B_in = _B_setup()

    return {
        "A_u": _A_in["u"],
        "A_log_A_real": _A_in["log_A_real"],
        "A_A_imag": _A_in["A_imag"],
        "A_C_real": _A_in["C_real"],
        "A_C_imag": _A_in["C_imag"],
        "A_log_dt": _A_in["log_dt"],
        "A_D_skip": _A_in["D_skip"],
        "B_x": _B_in["x"],
        "B_w1": _B_in["w1"],
        "B_b1": _B_in["b1"],
        "B_w2": _B_in["w2"],
        "B_b2": _B_in["b2"],
    }

def reference(A_u, A_log_A_real, A_A_imag, A_C_real, A_C_imag, A_log_dt, A_D_skip, B_x, B_w1, B_b1, B_w2, B_b2):
    def _A_ref(u, log_A_real, A_imag, C_real, C_imag, log_dt, D_skip):
        L = u.shape[1]
        dt = torch.exp(log_dt)
        A = -torch.exp(log_A_real) + 1j * A_imag
        C = (C_real + 1j * C_imag) * dt[:, None]
        pos = (lambda x, dt: x.to(dt))(torch.arange(L), log_dt.dtype)
        P = torch.exp((A * dt[:, None])[:, :, None] * pos[None, None, :])
        K = 2.0 * torch.einsum("dn,dnl->dl", C, P).real
        L2 = 2 * L
        u_t = (lambda x, *d: x.permute(*d))(u, 0, 2, 1)
        Kf = (lambda x, *a, **k: torch.fft.rfft(x.float(), *a, **k))(K, L2)
        Uf = (lambda x, *a, **k: torch.fft.rfft(x.float(), *a, **k))(u_t, L2)
        y = (lambda x, *a, **k: torch.fft.irfft(x, *a, **k).to(torch.bfloat16))(Uf * Kf[None, :, :], L2)[:, :, :L]
        y = (lambda x, *d: x.permute(*d))(y, 0, 2, 1)
        return y + u * D_skip[None, None, :]

    def _B_ref(x, w1, b1, w2, b2):
        h = torch.einsum("bsd,df->bsf", x, w1) + b1
        zero = torch.zeros_like(h)
        r = torch.where(h > zero, h, zero)
        h = r * r
        out = torch.einsum("bsf,fd->bsd", h, w2) + b2
        return out

    x = A_u
    h = x + _A_ref(_layernorm(x, axis=-1), A_log_A_real, A_A_imag, A_C_real, A_C_imag, A_log_dt, A_D_skip)
    h2 = h + _B_ref(_layernorm(h, axis=-1), B_w1, B_b1, B_w2, B_b2)
    return h2
```

```cpp
#include <hip/hip_runtime.h>
#include <hip/hip_bf16.h>
#include <stdint.h>

typedef unsigned u32;
typedef unsigned u32x2 __attribute__((ext_vector_type(2)));
typedef unsigned u32x4 __attribute__((ext_vector_type(4)));
typedef float f32x2 __attribute__((ext_vector_type(2)));
typedef float f32x4 __attribute__((ext_vector_type(4)));
typedef float f32x16 __attribute__((ext_vector_type(16)));
typedef short s16x4 __attribute__((ext_vector_type(4)));
typedef _Float16 f16x8 __attribute__((ext_vector_type(8)));
#define GAS __attribute__((address_space(1)))
#define LAS __attribute__((address_space(3)))

constexpr int L = 2048, D = 576, F = 4608, NM = 128;

constexpr size_t WS_WT   = 0;
constexpr size_t WS_WT_B = (size_t)8 * 4 * 324 * 1024;
constexpr size_t WS_CS   = WS_WT + WS_WT_B;
constexpr size_t WS_CNT  = WS_CS + 32768;
constexpr size_t WS_PART = WS_CNT + 131072;
constexpr size_t WS_PART_B = (size_t)32 * 8 * 8 * 9216;
#define TROW 4352
constexpr int    T_ROW   = TROW;
constexpr size_t WS_HT   = WS_PART + WS_PART_B;
constexpr size_t WS_XT   = WS_HT + (size_t)D * T_ROW;
constexpr size_t WS_ST   = WS_XT + (size_t)D * T_ROW;
constexpr int    G_ROW   = 4352;
constexpr size_t WS_G    = WS_ST + (size_t)L * 8;
constexpr size_t WS_GS   = WS_G + (size_t)D * G_ROW;
constexpr size_t WS_END  = WS_GS + 4096;

__device__ __forceinline__ u32 cvt_pk_bf16(float a, float b) {
  typedef __bf16 bf2 __attribute__((ext_vector_type(2)));
  f32x2 v = {a, b};
  return __builtin_bit_cast(u32, __builtin_convertvector(v, bf2));
}
__device__ __forceinline__ float bf_lo(u32 x) { return __builtin_bit_cast(float, x << 16); }
__device__ __forceinline__ float bf_hi(u32 x) { return __builtin_bit_cast(float, x & 0xffff0000u); }
__device__ __forceinline__ float rbf(float x) { return bf_lo(cvt_pk_bf16(x, x)); }
__device__ __forceinline__ u32x2 tr_read(u32 lds_addr) {
  s16x4 r = __builtin_amdgcn_ds_read_tr16_b64_v4i16((LAS s16x4*)lds_addr);
  return __builtin_bit_cast(u32x2, r);
}
__device__ __forceinline__ void glds16(const void* g, u32 lds_addr) {
  __builtin_amdgcn_global_load_lds((const GAS u32*)g, (LAS u32*)lds_addr, 16, 0, 0);
}
__device__ __forceinline__ void st16_wt(void* p, u32x4 v) {
  asm volatile("global_store_dwordx4 %0, %1, off sc1\n\ts_nop 1" :: "v"(p), "v"(v));
}
template <int CTRL> __device__ __forceinline__ float dpp_add(float x) {
  return x + __builtin_bit_cast(float, __builtin_amdgcn_update_dpp(0, __builtin_bit_cast(int, x), CTRL, 0xf, 0xf, true));
}
__device__ __forceinline__ float sum8(float x) { return dpp_add<0x141>(dpp_add<0x4E>(dpp_add<0xB1>(x))); }
__device__ __forceinline__ float sum64(float x) {
  x = dpp_add<0x140>(sum8(x));
  const int b = __builtin_bit_cast(int, x);
  return (__builtin_bit_cast(float, __builtin_amdgcn_readlane(b, 0)) + __builtin_bit_cast(float, __builtin_amdgcn_readlane(b, 16))) +
         (__builtin_bit_cast(float, __builtin_amdgcn_readlane(b, 32)) + __builtin_bit_cast(float, __builtin_amdgcn_readlane(b, 48)));
}
__device__ __forceinline__ float dot2bf(u32 a, u32 b, float c) {
  typedef __bf16 bf2 __attribute__((ext_vector_type(2)));
  return __builtin_amdgcn_fdot2_f32_bf16(__builtin_bit_cast(bf2, a), __builtin_bit_cast(bf2, b), c, false);
}

#define CODE_TOUCH(v, tag, B, E, i) { unsigned long long pc_; int d_; u32 n_; \
    asm volatile("s_getpc_b64 %0\n" tag ":\n\ts_mov_b32 %1, " B "-" tag "\n\ts_mov_b32 %2, " E "-" B "-4" : "=s"(pc_), "=s"(d_), "=s"(n_)); \
    if ((int)n_ < 0) n_ = 0; \
    const u32 o_ = (u32)(i) * 128u < n_ ? (u32)(i) * 128u : n_; \
    asm volatile("global_load_dword %0, %1, %2" : "=v"(v) : "v"(o_), "s"(pc_ + (long long)d_) : "memory"); }
#define KEEP(v, x) asm volatile("" :: "v"(v), "v"(x));
#define LABEL(x) asm volatile(x ":");

#define TL_BEGIN
#define TL_END(k)
#define TL_MID(k, i)

template <bool CS>
__device__ __forceinline__ void pretile(const char* src, u32 rs, char* wt, int s, int cb, int ph, float* cs) {
  const int tid = threadIdx.x, lane = tid & 63, w = __builtin_amdgcn_readfirstlane(tid >> 6);
  {
    const int r = lane >> 3, p = lane & 7;
    const u32 base = r * rs + (p & 1) * 16;
    const u32 c0 = ((p >> 1) ^ ((r >> 1) & 1)) * 32;
#pragma unroll
    for (int i = 0; i < 18; ++i) {
      const int row0 = 144 * w + 8 * i;
      glds16(src + (size_t)row0 * rs + base + (c0 ^ ((i & 1) * 64)), row0 * 128);
    }
  }
  __syncthreads();
  const int q = lane >> 4, a = (lane >> 2) & 3, pp = lane & 3;
  const u32 g = ((a >> 1) & 1) | ((q & 1) << 1);
  const u32 ta = (8 * q + a) * 128 + ((g ^ w) * 32) + pp * 8;
  const int t = 4 * cb + w;
  const int tp = t % 9, pos = ph == 0 ? tp : (tp == 8 ? 0 : ((tp - s - 1) & 7) + 1);
  char* dst = wt + ((size_t)((s * 4 + t / 9) * 324 + ph * 162 + pos * 18)) * 1024 + lane * 16;
  float sum = 0.f;
  u32x4 fr[18];
#pragma unroll
  for (int ks = 0; ks < 18; ++ks) {
    const u32x2 lo = tr_read(ta + ks * 4096), hi = tr_read(ta + ks * 4096 + 512);
    fr[ks] = u32x4{lo[0], lo[1], hi[0], hi[1]};
  }
#pragma unroll
  for (int ks = 0; ks < 18; ++ks) {
    const u32x4 v = fr[ks];
    st16_wt(dst + ks * 1024, v);
    if (CS) {
      sum = dot2bf(v[0], 0x3f803f80u, sum); sum = dot2bf(v[1], 0x3f803f80u, sum);
      sum = dot2bf(v[2], 0x3f803f80u, sum); sum = dot2bf(v[3], 0x3f803f80u, sum);
    }
  }
  if (CS) {
    sum += __shfl_xor(sum, 16);
    sum += __shfl_xor(sum, 32);
    if (lane < 16) cs[s * 576 + cb * 64 + w * 16 + lane] = sum;
  }
}

constexpr int K3_HBASE = 73728, K3_HSTR = 1184, K3_PBASE = K3_HBASE + 64 * K3_HSTR, K3_SBASE = K3_PBASE + 4608;
constexpr int K3_LDS = K3_SBASE + 512 + 64;
#define K3_NS 2

__global__ __launch_bounds__(256, 1)
void k3_mlp(const char* __restrict__ ws_c, char* __restrict__ ws, const unsigned short* __restrict__ b1,
            const unsigned short* __restrict__ b2, unsigned short* __restrict__ out) {
  TL_BEGIN
  const int tid = threadIdx.x;
  const int w = __builtin_amdgcn_readfirstlane(tid >> 6);
  const int xcd = blockIdx.x & 7, jb = blockIdx.x >> 3;
  const int s = K3_NS * (xcd % (8 / K3_NS)) + (jb % K3_NS);
  const int mt = (xcd / (8 / K3_NS)) * (32 / K3_NS) + jb / K3_NS;
  const int lane = tid & 63;
  const int q = lane >> 4, c = lane & 15, a = (lane >> 2) & 3, pp = lane & 3;
  const u32 vw0 = lane * 16;
  const int r = lane >> 3, p = lane & 7;
  const u32 vg0 = r * T_ROW + (((p >> 1) ^ (((r >> 1) & 1) | ((w & 1) << 1))) * 32) + (p & 1) * 16;
  const u32 g = ((a >> 1) & 1) | ((q & 1) << 1);
  const u32 vt0 = (8 * q + a) * 128 + g * 32 + pp * 8;
  const u32 phi = (c >> 2) & 1;
  const u32 vhw = K3_HBASE + c * K3_HSTR + 288 * w + 16 * ((q >> 1) ^ phi) + 8 * (q & 1);
  const u32 vhr = K3_HBASE + c * K3_HSTR + 16 * (q ^ phi);
  const u32 vp = K3_PBASE + (144 * w + 4 * q) * 4;
  const u32 vsr = K3_SBASE + c * 8;
  const u32 vsw = (q == (c >> 2)) ? K3_SBASE + (16 * w + c) * 8 : K3_SBASE + 512;
  const u32 vcl = c & 3;
  const u32 vpo = (tid < 143 ? tid : 143) * 16;
  const int cbo = 9 * w + s, cbx = 9 * (s >> 1) + 8, hf = s & 1;
  const u32 gg = ((a >> 1) & 1) | (((q >> 1) & 1) << 1);
  const u32 vho = (16 * cbo + 4 * q + a) * 128 + gg * 32 + pp * 8;
  const u32 vout = c * (D * 2) + 8 * q + cbo * 32, vb2 = 8 * q + cbo * 32;
  const u32 sdx = (u32)((cbx - cbo) * 32), shx = hf * 64;
  const u32 one = (q == (c >> 2)) ? ((c & 1) ? 0x3f800000u : 0x00003f80u) : 0u;
  const u32 vsx = (c & 2) ? 0u : one, vsy = (c & 2) ? one : 0u;
  const char* sw = ws_c + WS_WT + (size_t)((s * 4 + w) * 324) * 1024;
  const char* sht = ws_c + WS_HT + (size_t)(8 * w) * T_ROW + mt * 128;
  char* spart = ws + WS_PART + (size_t)((mt * 8 + 2 * w) * 8 + s) * 9216 + 8192;
  char* sbase = ws + WS_PART + (size_t)(mt * 64 + s) * 9216 + w * 2048;
  const char* sown = ws_c + WS_PART + (size_t)((mt * 8 + s) * 8) * 9216 + w * 2048;
  char* sfl = ws + WS_CNT + mt * 4096;
  const char* sfe = ws_c + WS_CNT + ((mt * 8 + s) * 4 + w) * 128;
  const u32 srot = ((s + 1) & 7) * 73728, ss = s, sxoff = 8192 - w * 2048;
  const char* scs = ws_c + WS_CS + s * 576 * 4;
  const char* sb1 = (const char*)b1 + s * 576 * 2;
  const char* sb2 = (const char*)b2;
  char* sout = (char*)out + (size_t)(mt * 64) * (D * 2);
  const u32 sm0 = w * 1024, swx = w * 32, swv = w;
  asm volatile(
    "K3_CODE_B:\n"
    "v_add_u32 v16, 0xc00, %[vw0]\n"
    "v_mov_b32 v17, %[vw0]\n"
    "v_mov_b32 v18, %[vg0]\n"
    "v_mov_b32 v19, %[vcl]\n"
    "v_xor_b32 v20, 0, %[vt0]\n"
    "v_add_u32 v24, 0x10000, v20\n"
    "v_xor_b32 v21, 32, %[vt0]\n"
    "v_add_u32 v25, 0x10000, v21\n"
    "v_xor_b32 v22, 64, %[vt0]\n"
    "v_add_u32 v26, 0x10000, v22\n"
    "v_xor_b32 v23, 96, %[vt0]\n"
    "v_add_u32 v27, 0x10000, v23\n"
    "v_xor_b32 v28, %[swx], %[vt0]\n"
    "v_add_u32 v29, 0x10000, v28\n"
    "v_mov_b32 v30, %[vhw]\n"
    "v_mov_b32 v31, %[vhr]\n"
    "v_mov_b32 v32, %[vp]\n"
    "v_mov_b32 v33, %[vsr]\n"
    "v_mov_b32 v34, %[vsw]\n"
    "s_mov_b64 s[36:37], %[sw]\n"
    "s_mov_b64 s[38:39], %[sht]\n"
    "s_mov_b64 s[40:41], %[spart]\n"
    "s_add_u32 s42, s40, 0x12000\n"
    "s_addc_u32 s43, s41, 0\n"
    "s_mov_b64 s[44:45], %[sfl]\n"
    "s_mov_b32 s47, %[sm0]\n"
    "s_getpc_b64 s[62:63]\n"
    "K3PC_%=:\n"
    "s_lshl_b32 s61, %[swv], 13\n"
    "v_lshlrev_b32 v52, 3, %[vw0]\n"
    "v_add_u32 v52, s61, v52\n"
    "v_min_u32 v52, K3END_%=-K3PC_%=-4, v52\n"
    "global_load_dword v53, v52, s[62:63]\n"
    "v_lshrrev_b32 v74, 1, %[vpo]\n"
    "global_load_dwordx4 v[88:91], %[vpo], %[scs]\n"
    "global_load_dwordx2 v[92:93], v74, %[sb1]\n"
    "v_add_u32 v78, 18432, v16\n"
    "s_mov_b32 m0, s47\n"
    "s_add_u32 s47, s47, 0x1000\n"
    "global_load_lds_dwordx4 v18, s[38:39]\n"
    "s_add_u32 s38, s38, 139264\n"
    "s_addc_u32 s39, s39, 0\n"
    "s_mov_b32 m0, s47\n"
    "s_add_u32 s47, s47, 0x1000\n"
    "global_load_lds_dwordx4 v18, s[38:39]\n"
    "s_add_u32 s38, s38, 139264\n"
    "s_addc_u32 s39, s39, 0\n"
    "s_mov_b32 m0, s47\n"
    "s_add_u32 s47, s47, 0x1000\n"
    "global_load_lds_dwordx4 v18, s[38:39]\n"
    "s_add_u32 s38, s38, 139264\n"
    "s_addc_u32 s39, s39, 0\n"
    "s_mov_b32 m0, s47\n"
    "s_add_u32 s47, s47, 0x1000\n"
    "global_load_lds_dwordx4 v18, s[38:39]\n"
    "s_add_u32 s38, s38, 139264\n"
    "s_addc_u32 s39, s39, 0\n"
    "s_mov_b32 m0, s47\n"
    "s_add_u32 s47, s47, 0x1000\n"
    "global_load_lds_dwordx4 v18, s[38:39]\n"
    "s_add_u32 s38, s38, 139264\n"
    "s_addc_u32 s39, s39, 0\n"
    "s_mov_b32 m0, s47\n"
    "s_add_u32 s47, s47, 0x1000\n"
    "global_load_lds_dwordx4 v18, s[38:39]\n"
    "s_add_u32 s38, s38, 139264\n"
    "s_addc_u32 s39, s39, 0\n"
    "global_load_dwordx4 v[152:155], v16, s[36:37] offset:-3072\n"
    "global_load_dwordx4 v[156:159], v16, s[36:37] offset:-2048\n"
    "global_load_dwordx4 v[160:163], v16, s[36:37] offset:-1024\n"
    "global_load_dwordx4 v[164:167], v16, s[36:37] offset:0\n"
    "global_load_dwordx4 v[168:171], v16, s[36:37] offset:1024\n"
    "global_load_dwordx4 v[172:175], v16, s[36:37] offset:2048\n"
    "v_add_u32 v16, 0x1800, v16\n"
    "v_mov_b32 v108, 0x3f803f80\n"
    "v_mov_b32 v109, 0x3f803f80\n"
    "v_mov_b32 v110, 0x3f803f80\n"
    "v_mov_b32 v111, 0x3f803f80\n"
    "s_waitcnt vmcnt(12)\n"
    "v_lshlrev_b32 v94, 16, v92\n"
    "v_and_b32 v95, 0xffff0000, v92\n"
    "v_lshlrev_b32 v96, 16, v93\n"
    "v_and_b32 v97, 0xffff0000, v93\n"
    "v_add_u32 v74, 149504, %[vpo]\n"
    "ds_write_b128 v74, v[88:91]\n"
    "ds_write_b128 v74, v[94:97] offset:2304\n"
    "s_mov_b32 m0, s47\n"
    "s_add_u32 s47, s47, 0x1000\n"
    "global_load_lds_dwordx4 v18, s[38:39]\n"
    "s_add_u32 s38, s38, 139264\n"
    "s_addc_u32 s39, s39, 0\n"
    "s_mov_b32 m0, s47\n"
    "s_add_u32 s47, s47, 0x1000\n"
    "global_load_lds_dwordx4 v18, s[38:39]\n"
    "s_add_u32 s38, s38, 139264\n"
    "s_addc_u32 s39, s39, 0\n"
    "s_mov_b32 m0, s47\n"
    "s_add_u32 s47, s47, 0x1000\n"
    "global_load_lds_dwordx4 v18, s[38:39]\n"
    "s_add_u32 s38, s38, 139264\n"
    "s_addc_u32 s39, s39, 0\n"
    "s_mov_b32 m0, s47\n"
    "s_add_u32 s47, s47, 0x1000\n"
    "global_load_lds_dwordx4 v18, s[38:39]\n"
    "s_add_u32 s38, s38, 139264\n"
    "s_addc_u32 s39, s39, 0\n"
    "s_mov_b32 m0, s47\n"
    "s_add_u32 s47, s47, 0x1000\n"
    "global_load_lds_dwordx4 v18, s[38:39]\n"
    "s_add_u32 s38, s38, 139264\n"
    "s_addc_u32 s39, s39, 0\n"
    "s_mov_b32 m0, s47\n"
    "s_add_u32 s47, s47, 0x1000\n"
    "global_load_lds_dwordx4 v18, s[38:39]\n"
    "s_add_u32 s38, s38, 139264\n"
    "s_addc_u32 s39, s39, 0\n"
    "global_load_dwordx4 v[176:179], v16, s[36:37] offset:-3072\n"
    "global_load_dwordx4 v[180:183], v16, s[36:37] offset:-2048\n"
    "global_load_dwordx4 v[184:187], v16, s[36:37] offset:-1024\n"
    "global_load_dwordx4 v[188:191], v16, s[36:37] offset:0\n"
    "global_load_dwordx4 v[192:195], v16, s[36:37] offset:1024\n"
    "global_load_dwordx4 v[196:199], v16, s[36:37] offset:2048\n"
    "v_add_u32 v16, 0x1800, v16\n"
    "s_waitcnt vmcnt(12)\n"
    "s_waitcnt lgkmcnt(0)\n"
    "s_barrier\n"
    "ds_read_b64_tr_b16 a[0:1], v20 offset:0\n"
    "ds_read_b64_tr_b16 a[2:3], v20 offset:512\n"
    "ds_read_b64_tr_b16 a[4:5], v21 offset:0\n"
    "ds_read_b64_tr_b16 a[6:7], v21 offset:512\n"
    "ds_read_b64_tr_b16 a[8:9], v22 offset:0\n"
    "ds_read_b64_tr_b16 a[10:11], v22 offset:512\n"
    "ds_read_b64_tr_b16 a[12:13], v23 offset:0\n"
    "ds_read_b64_tr_b16 a[14:15], v23 offset:512\n"
    "ds_read_b64_tr_b16 v[64:65], v28 offset:0\n"
    "ds_read_b64_tr_b16 v[66:67], v28 offset:512\n"
    "ds_read_b128 v[36:39], v32\n"
    "ds_read_b128 v[40:43], v32 offset:2304\n"
    "v_add_u32 v32, 64, v32\n"
    "s_waitcnt lgkmcnt(8)\n"
    "v_mfma_f32_16x16x32_bf16 v[120:123], v[152:155], a[0:3], 0\n"
    "ds_read_b64_tr_b16 a[16:17], v20 offset:4096\n"
    "ds_read_b64_tr_b16 a[18:19], v20 offset:4608\n"
    "s_mov_b32 m0, s47\n"
    "s_add_u32 s47, s47, 0x1000\n"
    "global_load_lds_dwordx4 v18, s[38:39]\n"
    "s_add_u32 s38, s38, 139264\n"
    "s_addc_u32 s39, s39, 0\n"
    "v_mfma_f32_16x16x32_bf16 v[124:127], v[152:155], a[4:7], 0\n"
    "ds_read_b64_tr_b16 a[20:21], v21 offset:4096\n"
    "ds_read_b64_tr_b16 a[22:23], v21 offset:4608\n"
    "global_load_dwordx4 v[200:203], v16, s[36:37] offset:-3072\n"
    "s_waitcnt lgkmcnt(8)\n"
    "v_mfma_f32_16x16x32_bf16 v[128:131], v[152:155], a[8:11], 0\n"
    "ds_read_b64_tr_b16 a[24:25], v22 offset:4096\n"
    "ds_read_b64_tr_b16 a[26:27], v22 offset:4608\n"
    "s_mov_b32 m0, s47\n"
    "s_add_u32 s47, s47, 0x1000\n"
    "global_load_lds_dwordx4 v18, s[38:39]\n"
    "s_add_u32 s38, s38, 139264\n"
    "s_addc_u32 s39, s39, 0\n"
    "v_mfma_f32_16x16x32_bf16 v[132:135], v[152:155], a[12:15], 0\n"
    "ds_read_b64_tr_b16 a[28:29], v23 offset:4096\n"
    "ds_read_b64_tr_b16 a[30:31], v23 offset:4608\n"
    "global_load_dwordx4 v[204:207], v16, s[36:37] offset:-2048\n"
    "s_waitcnt lgkmcnt(10)\n"
    "v_mfma_f32_16x16x32_bf16 v[100:103], v[108:111], v[64:67], 0\n"
    "v_mfma_f32_16x16x32_bf16 v[104:107], v[64:67], v[64:67], 0\n"
    "ds_read_b64_tr_b16 v[68:69], v28 offset:4096\n"
    "ds_read_b64_tr_b16 v[70:71], v28 offset:4608\n"
    "s_waitcnt lgkmcnt(6)\n"
    "v_mfma_f32_16x16x32_bf16 v[120:123], v[156:159], a[16:19], v[120:123]\n"
    "ds_read_b64_tr_b16 a[32:33], v20 offset:8192\n"
    "ds_read_b64_tr_b16 a[34:35], v20 offset:8704\n"
    "s_mov_b32 m0, s47\n"
    "s_add_u32 s47, s47, 0x1000\n"
    "global_load_lds_dwordx4 v18, s[38:39]\n"
    "s_add_u32 s38, s38, 139264\n"
    "s_addc_u32 s39, s39, 0\n"
    "v_mfma_f32_16x16x32_bf16 v[124:127], v[156:159], a[20:23], v[124:127]\n"
    "ds_read_b64_tr_b16 a[36:37], v21 offset:8192\n"
    "ds_read_b64_tr_b16 a[38:39], v21 offset:8704\n"
    "global_load_dwordx4 v[208:211], v16, s[36:37] offset:-1024\n"
    "s_waitcnt lgkmcnt(6)\n"
    "v_mfma_f32_16x16x32_bf16 v[128:131], v[156:159], a[24:27], v[128:131]\n"
    "ds_read_b64_tr_b16 a[40:41], v22 offset:8192\n"
    "ds_read_b64_tr_b16 a[42:43], v22 offset:8704\n"
    "s_mov_b32 m0, s47\n"
    "s_add_u32 s47, s47, 0x1000\n"
    "global_load_lds_dwordx4 v18, s[38:39]\n"
    "s_add_u32 s38, s38, 139264\n"
    "s_addc_u32 s39, s39, 0\n"
    "v_mfma_f32_16x16x32_bf16 v[132:135], v[156:159], a[28:31], v[132:135]\n"
    "ds_read_b64_tr_b16 a[44:45], v23 offset:8192\n"
    "ds_read_b64_tr_b16 a[46:47], v23 offset:8704\n"
    "global_load_dwordx4 v[212:215], v16, s[36:37] offset:0\n"
    "s_waitcnt lgkmcnt(8)\n"
    "v_mfma_f32_16x16x32_bf16 v[100:103], v[108:111], v[68:71], v[100:103]\n"
    "v_mfma_f32_16x16x32_bf16 v[104:107], v[68:71], v[68:71], v[104:107]\n"
    "ds_read_b64_tr_b16 v[64:65], v28 offset:8192\n"
    "ds_read_b64_tr_b16 v[66:67], v28 offset:8704\n"
    "s_waitcnt lgkmcnt(6)\n"
    "v_mfma_f32_16x16x32_bf16 v[120:123], v[160:163], a[32:35], v[120:123]\n"
    "ds_read_b64_tr_b16 a[48:49], v20 offset:12288\n"
    "ds_read_b64_tr_b16 a[50:51], v20 offset:12800\n"
    "s_mov_b32 m0, s47\n"
    "s_add_u32 s47, s47, 0x1000\n"
    "global_load_lds_dwordx4 v18, s[38:39]\n"
    "s_add_u32 s38, s38, 139264\n"
    "s_addc_u32 s39, s39, 0\n"
    "v_mfma_f32_16x16x32_bf16 v[124:127], v[160:163], a[36:39], v[124:127]\n"
    "ds_read_b64_tr_b16 a[52:53], v21 offset:12288\n"
    "ds_read_b64_tr_b16 a[54:55], v21 offset:12800\n"
    "global_load_dwordx4 v[216:219], v16, s[36:37] offset:1024\n"
    "s_waitcnt lgkmcnt(6)\n"
    "v_mfma_f32_16x16x32_bf16 v[128:131], v[160:163], a[40:43], v[128:131]\n"
    "ds_read_b64_tr_b16 a[56:57], v22 offset:12288\n"
    "ds_read_b64_tr_b16 a[58:59], v22 offset:12800\n"
    "s_mov_b32 m0, s47\n"
    "s_add_u32 s47, s47, 0x1000\n"
    "global_load_lds_dwordx4 v18, s[38:39]\n"
    "s_add_u32 s38, s38, 139264\n"
    "s_addc_u32 s39, s39, 0\n"
    "v_mfma_f32_16x16x32_bf16 v[132:135], v[160:163], a[44:47], v[132:135]\n"
    "ds_read_b64_tr_b16 a[60:61], v23 offset:12288\n"
    "ds_read_b64_tr_b16 a[62:63], v23 offset:12800\n"
    "global_load_dwordx4 v[220:223], v16, s[36:37] offset:2048\n"
    "v_add_u32 v16, 0x1800, v16\n"
    "s_waitcnt lgkmcnt(8)\n"
    "v_mfma_f32_16x16x32_bf16 v[100:103], v[108:111], v[64:67], v[100:103]\n"
    "v_mfma_f32_16x16x32_bf16 v[104:107], v[64:67], v[64:67], v[104:107]\n"
    "ds_read_b64_tr_b16 v[68:69], v28 offset:12288\n"
    "ds_read_b64_tr_b16 v[70:71], v28 offset:12800\n"
    "s_waitcnt lgkmcnt(6)\n"
    "v_mfma_f32_16x16x32_bf16 v[120:123], v[164:167], a[48:51], v[120:123]\n"
    "ds_read_b64_tr_b16 a[64:65], v20 offset:16384\n"
    "ds_read_b64_tr_b16 a[66:67], v20 offset:16896\n"
    "v_mfma_f32_16x16x32_bf16 v[124:127], v[164:167], a[52:55], v[124:127]\n"
    "ds_read_b64_tr_b16 a[68:69], v21 offset:16384\n"
    "ds_read_b64_tr_b16 a[70:71], v21 offset:16896\n"
    "s_waitcnt lgkmcnt(6)\n"
    "v_mfma_f32_16x16x32_bf16 v[128:131], v[164:167], a[56:59], v[128:131]\n"
    "ds_read_b64_tr_b16 a[72:73], v22 offset:16384\n"
    "ds_read_b64_tr_b16 a[74:75], v22 offset:16896\n"
    "v_mfma_f32_16x16x32_bf16 v[132:135], v[164:167], a[60:63], v[132:135]\n"
    "ds_read_b64_tr_b16 a[76:77], v23 offset:16384\n"
    "ds_read_b64_tr_b16 a[78:79], v23 offset:16896\n"
    "s_waitcnt lgkmcnt(8)\n"
    "v_mfma_f32_16x16x32_bf16 v[100:103], v[108:111], v[68:71], v[100:103]\n"
    "v_mfma_f32_16x16x32_bf16 v[104:107], v[68:71], v[68:71], v[104:107]\n"
    "ds_read_b64_tr_b16 v[64:65], v28 offset:16384\n"
    "ds_read_b64_tr_b16 v[66:67], v28 offset:16896\n"
    "global_load_dwordx4 v[152:155], v78, s[36:37] offset:-3072\n"
    "global_load_dwordx4 v[156:159], v78, s[36:37] offset:-2048\n"
    "s_waitcnt lgkmcnt(6)\n"
    "v_mfma_f32_16x16x32_bf16 v[120:123], v[168:171], a[64:67], v[120:123]\n"
    "ds_read_b64_tr_b16 a[80:81], v20 offset:20480\n"
    "ds_read_b64_tr_b16 a[82:83], v20 offset:20992\n"
    "v_mfma_f32_16x16x32_bf16 v[124:127], v[168:171], a[68:71], v[124:127]\n"
    "ds_read_b64_tr_b16 a[84:85], v21 offset:20480\n"
    "ds_read_b64_tr_b16 a[86:87], v21 offset:20992\n"
    "s_waitcnt lgkmcnt(6)\n"
    "v_mfma_f32_16x16x32_bf16 v[128:131], v[168:171], a[72:75], v[128:131]\n"
    "ds_read_b64_tr_b16 a[88:89], v22 offset:20480\n"
    "ds_read_b64_tr_b16 a[90:91], v22 offset:20992\n"
    "v_mfma_f32_16x16x32_bf16 v[132:135], v[168:171], a[76:79], v[132:135]\n"
    "ds_read_b64_tr_b16 a[92:93], v23 offset:20480\n"
    "ds_read_b64_tr_b16 a[94:95], v23 offset:20992\n"
    "s_waitcnt lgkmcnt(8)\n"
    "v_mfma_f32_16x16x32_bf16 v[100:103], v[108:111], v[64:67], v[100:103]\n"
    "v_mfma_f32_16x16x32_bf16 v[104:107], v[64:67], v[64:67], v[104:107]\n"
    "ds_read_b64_tr_b16 v[68:69], v28 offset:20480\n"
    "ds_read_b64_tr_b16 v[70:71], v28 offset:20992\n"
    "global_load_dwordx4 v[160:163], v78, s[36:37] offset:-1024\n"
    "global_load_dwordx4 v[164:167], v78, s[36:37] offset:0\n"
    "s_waitcnt lgkmcnt(6)\n"
    "v_mfma_f32_16x16x32_bf16 v[120:123], v[172:175], a[80:83], v[120:123]\n"
    "v_mfma_f32_16x16x32_bf16 v[124:127], v[172:175], a[84:87], v[124:127]\n"
    "s_waitcnt lgkmcnt(2)\n"
    "v_mfma_f32_16x16x32_bf16 v[128:131], v[172:175], a[88:91], v[128:131]\n"
    "v_mfma_f32_16x16x32_bf16 v[132:135], v[172:175], a[92:95], v[132:135]\n"
    "s_waitcnt lgkmcnt(0)\n"
    "v_mfma_f32_16x16x32_bf16 v[100:103], v[108:111], v[68:71], v[100:103]\n"
    "v_mfma_f32_16x16x32_bf16 v[104:107], v[68:71], v[68:71], v[104:107]\n"
    "global_load_dwordx4 v[168:171], v78, s[36:37] offset:1024\n"
    "global_load_dwordx4 v[172:175], v78, s[36:37] offset:2048\n"
    "v_add_u32 v78, 0x1800, v78\n"
    "s_waitcnt vmcnt(18)\n"
    "s_waitcnt lgkmcnt(0)\n"
    "s_barrier\n"
    "ds_read_b64_tr_b16 a[96:97], v20 offset:24576\n"
    "ds_read_b64_tr_b16 a[98:99], v20 offset:25088\n"
    "ds_read_b64_tr_b16 a[100:101], v21 offset:24576\n"
    "ds_read_b64_tr_b16 a[102:103], v21 offset:25088\n"
    "ds_read_b64_tr_b16 a[104:105], v22 offset:24576\n"
    "ds_read_b64_tr_b16 a[106:107], v22 offset:25088\n"
    "ds_read_b64_tr_b16 a[108:109], v23 offset:24576\n"
    "ds_read_b64_tr_b16 a[110:111], v23 offset:25088\n"
    "ds_read_b64_tr_b16 v[64:65], v28 offset:24576\n"
    "ds_read_b64_tr_b16 v[66:67], v28 offset:25088\n"
    "s_waitcnt lgkmcnt(6)\n"
    "v_mfma_f32_16x16x32_bf16 v[120:123], v[176:179], a[96:99], v[120:123]\n"
    "ds_read_b64_tr_b16 a[112:113], v20 offset:28672\n"
    "ds_read_b64_tr_b16 a[114:115], v20 offset:29184\n"
    "v_mfma_f32_16x16x32_bf16 v[124:127], v[176:179], a[100:103], v[124:127]\n"
    "ds_read_b64_tr_b16 a[116:117], v21 offset:28672\n"
    "ds_read_b64_tr_b16 a[118:119], v21 offset:29184\n"
    "s_waitcnt lgkmcnt(6)\n"
    "v_mfma_f32_16x16x32_bf16 v[128:131], v[176:179], a[104:107], v[128:131]\n"
    "ds_read_b64_tr_b16 a[120:121], v22 offset:28672\n"
    "ds_read_b64_tr_b16 a[122:123], v22 offset:29184\n"
    "v_mfma_f32_16x16x32_bf16 v[132:135], v[176:179], a[108:111], v[132:135]\n"
    "ds_read_b64_tr_b16 a[124:125], v23 offset:28672\n"
    "ds_read_b64_tr_b16 a[126:127], v23 offset:29184\n"
    "s_waitcnt lgkmcnt(8)\n"
    "v_mfma_f32_16x16x32_bf16 v[100:103], v[108:111], v[64:67], v[100:103]\n"
    "v_mfma_f32_16x16x32_bf16 v[104:107], v[64:67], v[64:67], v[104:107]\n"
    "ds_read_b64_tr_b16 v[68:69], v28 offset:28672\n"
    "ds_read_b64_tr_b16 v[70:71], v28 offset:29184\n"
    "global_load_dwordx4 v[176:179], v78, s[36:37] offset:-3072\n"
    "s_waitcnt lgkmcnt(6)\n"
    "v_mfma_f32_16x16x32_bf16 v[120:123], v[180:183], a[112:115], v[120:123]\n"
    "ds_read_b64_tr_b16 a[128:129], v20 offset:32768\n"
    "ds_read_b64_tr_b16 a[130:131], v20 offset:33280\n"
    "v_mfma_f32_16x16x32_bf16 v[124:127], v[180:183], a[116:119], v[124:127]\n"
    "ds_read_b64_tr_b16 a[132:133], v21 offset:32768\n"
    "ds_read_b64_tr_b16 a[134:135], v21 offset:33280\n"
    "s_waitcnt lgkmcnt(6)\n"
    "v_mfma_f32_16x16x32_bf16 v[128:131], v[180:183], a[120:123], v[128:131]\n"
    "ds_read_b64_tr_b16 a[136:137], v22 offset:32768\n"
    "ds_read_b64_tr_b16 a[138:139], v22 offset:33280\n"
    "v_mfma_f32_16x16x32_bf16 v[132:135], v[180:183], a[124:127], v[132:135]\n"
    "ds_read_b64_tr_b16 a[140:141], v23 offset:32768\n"
    "ds_read_b64_tr_b16 a[142:143], v23 offset:33280\n"
    "s_waitcnt lgkmcnt(8)\n"
    "v_mfma_f32_16x16x32_bf16 v[100:103], v[108:111], v[68:71], v[100:103]\n"
    "v_mfma_f32_16x16x32_bf16 v[104:107], v[68:71], v[68:71], v[104:107]\n"
    "ds_read_b64_tr_b16 v[64:65], v28 offset:32768\n"
    "ds_read_b64_tr_b16 v[66:67], v28 offset:33280\n"
    "global_load_dwordx4 v[180:183], v78, s[36:37] offset:-2048\n"
    "s_waitcnt lgkmcnt(6)\n"
    "v_mfma_f32_16x16x32_bf16 v[120:123], v[184:187], a[128:131], v[120:123]\n"
    "ds_read_b64_tr_b16 a[144:145], v20 offset:36864\n"
    "ds_read_b64_tr_b16 a[146:147], v20 offset:37376\n"
    "v_mfma_f32_16x16x32_bf16 v[124:127], v[184:187], a[132:135], v[124:127]\n"
    "ds_read_b64_tr_b16 a[148:149], v21 offset:36864\n"
    "ds_read_b64_tr_b16 a[150:151], v21 offset:37376\n"
    "s_waitcnt lgkmcnt(6)\n"
    "v_mfma_f32_16x16x32_bf16 v[128:131], v[184:187], a[136:139], v[128:131]\n"
    "ds_read_b64_tr_b16 a[152:153], v22 offset:36864\n"
    "ds_read_b64_tr_b16 a[154:155], v22 offset:37376\n"
    "v_mfma_f32_16x16x32_bf16 v[132:135], v[184:187], a[140:143], v[132:135]\n"
    "ds_read_b64_tr_b16 a[156:157], v23 offset:36864\n"
    "ds_read_b64_tr_b16 a[158:159], v23 offset:37376\n"
    "s_waitcnt lgkmcnt(8)\n"
    "v_mfma_f32_16x16x32_bf16 v[100:103], v[108:111], v[64:67], v[100:103]\n"
    "v_mfma_f32_16x16x32_bf16 v[104:107], v[64:67], v[64:67], v[104:107]\n"
    "ds_read_b64_tr_b16 v[68:69], v28 offset:36864\n"
    "ds_read_b64_tr_b16 v[70:71], v28 offset:37376\n"
    "global_load_dwordx4 v[184:187], v78, s[36:37] offset:-1024\n"
    "s_waitcnt lgkmcnt(6)\n"
    "v_mfma_f32_16x16x32_bf16 v[120:123], v[188:191], a[144:147], v[120:123]\n"
    "ds_read_b64_tr_b16 a[160:161], v20 offset:40960\n"
    "ds_read_b64_tr_b16 a[162:163], v20 offset:41472\n"
    "v_mfma_f32_16x16x32_bf16 v[124:127], v[188:191], a[148:151], v[124:127]\n"
    "ds_read_b64_tr_b16 a[164:165], v21 offset:40960\n"
    "ds_read_b64_tr_b16 a[166:167], v21 offset:41472\n"
    "s_waitcnt lgkmcnt(6)\n"
    "v_mfma_f32_16x16x32_bf16 v[128:131], v[188:191], a[152:155], v[128:131]\n"
    "ds_read_b64_tr_b16 a[168:169], v22 offset:40960\n"
    "ds_read_b64_tr_b16 a[170:171], v22 offset:41472\n"
    "v_mfma_f32_16x16x32_bf16 v[132:135], v[188:191], a[156:159], v[132:135]\n"
    "ds_read_b64_tr_b16 a[172:173], v23 offset:40960\n"
    "ds_read_b64_tr_b16 a[174:175], v23 offset:41472\n"
    "s_waitcnt lgkmcnt(8)\n"
    "v_mfma_f32_16x16x32_bf16 v[100:103], v[108:111], v[68:71], v[100:103]\n"
    "v_mfma_f32_16x16x32_bf16 v[104:107], v[68:71], v[68:71], v[104:107]\n"
    "ds_read_b64_tr_b16 v[64:65], v28 offset:40960\n"
    "ds_read_b64_tr_b16 v[66:67], v28 offset:41472\n"
    "global_load_dwordx4 v[188:191], v78, s[36:37] offset:0\n"
    "s_waitcnt lgkmcnt(6)\n"
    "v_mfma_f32_16x16x32_bf16 v[120:123], v[192:195], a[160:163], v[120:123]\n"
    "ds_read_b64_tr_b16 a[176:177], v20 offset:45056\n"
    "ds_read_b64_tr_b16 a[178:179], v20 offset:45568\n"
    "v_mfma_f32_16x16x32_bf16 v[124:127], v[192:195], a[164:167], v[124:127]\n"
    "ds_read_b64_tr_b16 a[180:181], v21 offset:45056\n"
    "ds_read_b64_tr_b16 a[182:183], v21 offset:45568\n"
    "s_waitcnt lgkmcnt(6)\n"
    "v_mfma_f32_16x16x32_bf16 v[128:131], v[192:195], a[168:171], v[128:131]\n"
    "ds_read_b64_tr_b16 a[184:185], v22 offset:45056\n"
    "ds_read_b64_tr_b16 a[186:187], v22 offset:45568\n"
    "v_mfma_f32_16x16x32_bf16 v[132:135], v[192:195], a[172:175], v[132:135]\n"
    "ds_read_b64_tr_b16 a[188:189], v23 offset:45056\n"
    "ds_read_b64_tr_b16 a[190:191], v23 offset:45568\n"
    "s_waitcnt lgkmcnt(8)\n"
    "v_mfma_f32_16x16x32_bf16 v[100:103], v[108:111], v[64:67], v[100:103]\n"
    "v_mfma_f32_16x16x32_bf16 v[104:107], v[64:67], v[64:67], v[104:107]\n"
    "ds_read_b64_tr_b16 v[68:69], v28 offset:45056\n"
    "ds_read_b64_tr_b16 v[70:71], v28 offset:45568\n"
    "global_load_dwordx4 v[192:195], v78, s[36:37] offset:1024\n"
    "s_waitcnt lgkmcnt(6)\n"
    "v_mfma_f32_16x16x32_bf16 v[120:123], v[196:199], a[176:179], v[120:123]\n"
    "v_mfma_f32_16x16x32_bf16 v[124:127], v[196:199], a[180:183], v[124:127]\n"
    "s_waitcnt lgkmcnt(2)\n"
    "v_mfma_f32_16x16x32_bf16 v[128:131], v[196:199], a[184:187], v[128:131]\n"
    "v_mfma_f32_16x16x32_bf16 v[132:135], v[196:199], a[188:191], v[132:135]\n"
    "s_waitcnt lgkmcnt(0)\n"
    "v_mfma_f32_16x16x32_bf16 v[100:103], v[108:111], v[68:71], v[100:103]\n"
    "v_mfma_f32_16x16x32_bf16 v[104:107], v[68:71], v[68:71], v[104:107]\n"
    "global_load_dwordx4 v[196:199], v78, s[36:37] offset:2048\n"
    "v_add_u32 v78, 0x1800, v78\n"
    "s_waitcnt vmcnt(12)\n"
    "s_waitcnt lgkmcnt(0)\n"
    "s_barrier\n"
    "ds_read_b64_tr_b16 a[192:193], v20 offset:49152\n"
    "ds_read_b64_tr_b16 a[194:195], v20 offset:49664\n"
    "ds_read_b64_tr_b16 a[196:197], v21 offset:49152\n"
    "ds_read_b64_tr_b16 a[198:199], v21 offset:49664\n"
    "ds_read_b64_tr_b16 a[200:201], v22 offset:49152\n"
    "ds_read_b64_tr_b16 a[202:203], v22 offset:49664\n"
    "ds_read_b64_tr_b16 a[204:205], v23 offset:49152\n"
    "ds_read_b64_tr_b16 a[206:207], v23 offset:49664\n"
    "ds_read_b64_tr_b16 v[64:65], v28 offset:49152\n"
    "ds_read_b64_tr_b16 v[66:67], v28 offset:49664\n"
    "s_waitcnt lgkmcnt(6)\n"
    "v_mfma_f32_16x16x32_bf16 v[120:123], v[200:203], a[192:195], v[120:123]\n"
    "ds_read_b64_tr_b16 a[208:209], v20 offset:53248\n"
    "ds_read_b64_tr_b16 a[210:211], v20 offset:53760\n"
    "v_mfma_f32_16x16x32_bf16 v[124:127], v[200:203], a[196:199], v[124:127]\n"
    "ds_read_b64_tr_b16 a[212:213], v21 offset:53248\n"
    "ds_read_b64_tr_b16 a[214:215], v21 offset:53760\n"
    "s_waitcnt lgkmcnt(6)\n"
    "v_mfma_f32_16x16x32_bf16 v[128:131], v[200:203], a[200:203], v[128:131]\n"
    "ds_read_b64_tr_b16 a[216:217], v22 offset:53248\n"
    "ds_read_b64_tr_b16 a[218:219], v22 offset:53760\n"
    "v_mfma_f32_16x16x32_bf16 v[132:135], v[200:203], a[204:207], v[132:135]\n"
    "ds_read_b64_tr_b16 a[220:221], v23 offset:53248\n"
    "ds_read_b64_tr_b16 a[222:223], v23 offset:53760\n"
    "s_waitcnt lgkmcnt(8)\n"
    "v_mfma_f32_16x16x32_bf16 v[100:103], v[108:111], v[64:67], v[100:103]\n"
    "v_mfma_f32_16x16x32_bf16 v[104:107], v[64:67], v[64:67], v[104:107]\n"
    "ds_read_b64_tr_b16 v[68:69], v28 offset:53248\n"
    "ds_read_b64_tr_b16 v[70:71], v28 offset:53760\n"
    "global_load_dwordx4 v[200:203], v78, s[36:37] offset:-3072\n"
    "s_waitcnt lgkmcnt(6)\n"
    "v_mfma_f32_16x16x32_bf16 v[120:123], v[204:207], a[208:211], v[120:123]\n"
    "ds_read_b64_tr_b16 a[224:225], v20 offset:57344\n"
    "ds_read_b64_tr_b16 a[226:227], v20 offset:57856\n"
    "v_mfma_f32_16x16x32_bf16 v[124:127], v[204:207], a[212:215], v[124:127]\n"
    "ds_read_b64_tr_b16 a[228:229], v21 offset:57344\n"
    "ds_read_b64_tr_b16 a[230:231], v21 offset:57856\n"
    "s_waitcnt lgkmcnt(6)\n"
    "v_mfma_f32_16x16x32_bf16 v[128:131], v[204:207], a[216:219], v[128:131]\n"
    "ds_read_b64_tr_b16 a[232:233], v22 offset:57344\n"
    "ds_read_b64_tr_b16 a[234:235], v22 offset:57856\n"
    "v_mfma_f32_16x16x32_bf16 v[132:135], v[204:207], a[220:223], v[132:135]\n"
    "ds_read_b64_tr_b16 a[236:237], v23 offset:57344\n"
    "ds_read_b64_tr_b16 a[238:239], v23 offset:57856\n"
    "s_waitcnt lgkmcnt(8)\n"
    "v_mfma_f32_16x16x32_bf16 v[100:103], v[108:111], v[68:71], v[100:103]\n"
    "v_mfma_f32_16x16x32_bf16 v[104:107], v[68:71], v[68:71], v[104:107]\n"
    "ds_read_b64_tr_b16 v[64:65], v28 offset:57344\n"
    "ds_read_b64_tr_b16 v[66:67], v28 offset:57856\n"
    "global_load_dwordx4 v[204:207], v78, s[36:37] offset:-2048\n"
    "s_waitcnt lgkmcnt(6)\n"
    "v_mfma_f32_16x16x32_bf16 v[120:123], v[208:211], a[224:227], v[120:123]\n"
    "ds_read_b64_tr_b16 a[240:241], v20 offset:61440\n"
    "ds_read_b64_tr_b16 a[242:243], v20 offset:61952\n"
    "v_mfma_f32_16x16x32_bf16 v[124:127], v[208:211], a[228:231], v[124:127]\n"
    "ds_read_b64_tr_b16 a[244:245], v21 offset:61440\n"
    "ds_read_b64_tr_b16 a[246:247], v21 offset:61952\n"
    "s_waitcnt lgkmcnt(6)\n"
    "v_mfma_f32_16x16x32_bf16 v[128:131], v[208:211], a[232:235], v[128:131]\n"
    "ds_read_b64_tr_b16 a[248:249], v22 offset:61440\n"
    "ds_read_b64_tr_b16 a[250:251], v22 offset:61952\n"
    "v_mfma_f32_16x16x32_bf16 v[132:135], v[208:211], a[236:239], v[132:135]\n"
    "ds_read_b64_tr_b16 a[252:253], v23 offset:61440\n"
    "ds_read_b64_tr_b16 a[254:255], v23 offset:61952\n"
    "s_waitcnt lgkmcnt(8)\n"
    "v_mfma_f32_16x16x32_bf16 v[100:103], v[108:111], v[64:67], v[100:103]\n"
    "v_mfma_f32_16x16x32_bf16 v[104:107], v[64:67], v[64:67], v[104:107]\n"
    "ds_read_b64_tr_b16 v[68:69], v28 offset:61440\n"
    "ds_read_b64_tr_b16 v[70:71], v28 offset:61952\n"
    "global_load_dwordx4 v[208:211], v78, s[36:37] offset:-1024\n"
    "s_waitcnt lgkmcnt(6)\n"
    "v_mfma_f32_16x16x32_bf16 v[120:123], v[212:215], a[240:243], v[120:123]\n"
    "ds_read_b64_tr_b16 v[224:225], v24 offset:0\n"
    "ds_read_b64_tr_b16 v[226:227], v24 offset:512\n"
    "v_mfma_f32_16x16x32_bf16 v[124:127], v[212:215], a[244:247], v[124:127]\n"
    "ds_read_b64_tr_b16 v[228:229], v25 offset:0\n"
    "ds_read_b64_tr_b16 v[230:231], v25 offset:512\n"
    "s_waitcnt lgkmcnt(6)\n"
    "v_mfma_f32_16x16x32_bf16 v[128:131], v[212:215], a[248:251], v[128:131]\n"
    "ds_read_b64_tr_b16 v[232:233], v26 offset:0\n"
    "ds_read_b64_tr_b16 v[234:235], v26 offset:512\n"
    "v_mfma_f32_16x16x32_bf16 v[132:135], v[212:215], a[252:255], v[132:135]\n"
    "ds_read_b64_tr_b16 v[236:237], v27 offset:0\n"
    "ds_read_b64_tr_b16 v[238:239], v27 offset:512\n"
    "s_waitcnt lgkmcnt(8)\n"
    "v_mfma_f32_16x16x32_bf16 v[100:103], v[108:111], v[68:71], v[100:103]\n"
    "v_mfma_f32_16x16x32_bf16 v[104:107], v[68:71], v[68:71], v[104:107]\n"
    "ds_read_b64_tr_b16 v[64:65], v29 offset:0\n"
    "ds_read_b64_tr_b16 v[66:67], v29 offset:512\n"
    "global_load_dwordx4 v[212:215], v78, s[36:37] offset:0\n"
    "s_waitcnt lgkmcnt(6)\n"
    "v_mfma_f32_16x16x32_bf16 v[120:123], v[216:219], v[224:227], v[120:123]\n"
    "ds_read_b64_tr_b16 v[240:241], v24 offset:4096\n"
    "ds_read_b64_tr_b16 v[242:243], v24 offset:4608\n"
    "v_mfma_f32_16x16x32_bf16 v[124:127], v[216:219], v[228:231], v[124:127]\n"
    "ds_read_b64_tr_b16 v[244:245], v25 offset:4096\n"
    "ds_read_b64_tr_b16 v[246:247], v25 offset:4608\n"
    "s_waitcnt lgkmcnt(6)\n"
    "v_mfma_f32_16x16x32_bf16 v[128:131], v[216:219], v[232:235], v[128:131]\n"
    "ds_read_b64_tr_b16 v[248:249], v26 offset:4096\n"
    "ds_read_b64_tr_b16 v[250:251], v26 offset:4608\n"
    "v_mfma_f32_16x16x32_bf16 v[132:135], v[216:219], v[236:239], v[132:135]\n"
    "ds_read_b64_tr_b16 v[252:253], v27 offset:4096\n"
    "ds_read_b64_tr_b16 v[254:255], v27 offset:4608\n"
    "s_waitcnt lgkmcnt(8)\n"
    "v_mfma_f32_16x16x32_bf16 v[100:103], v[108:111], v[64:67], v[100:103]\n"
    "v_mfma_f32_16x16x32_bf16 v[104:107], v[64:67], v[64:67], v[104:107]\n"
    "ds_read_b64_tr_b16 v[68:69], v29 offset:4096\n"
    "ds_read_b64_tr_b16 v[70:71], v29 offset:4608\n"
    "global_load_dwordx4 v[216:219], v78, s[36:37] offset:1024\n"
    "s_waitcnt lgkmcnt(6)\n"
    "v_mfma_f32_16x16x32_bf16 v[120:123], v[220:223], v[240:243], v[120:123]\n"
    "v_mfma_f32_16x16x32_bf16 v[124:127], v[220:223], v[244:247], v[124:127]\n"
    "s_waitcnt lgkmcnt(2)\n"
    "v_mfma_f32_16x16x32_bf16 v[128:131], v[220:223], v[248:251], v[128:131]\n"
    "v_mfma_f32_16x16x32_bf16 v[132:135], v[220:223], v[252:255], v[132:135]\n"
    "s_waitcnt lgkmcnt(0)\n"
    "v_mfma_f32_16x16x32_bf16 v[100:103], v[108:111], v[68:71], v[100:103]\n"
    "v_mfma_f32_16x16x32_bf16 v[104:107], v[68:71], v[68:71], v[104:107]\n"
    "global_load_dwordx4 v[220:223], v78, s[36:37] offset:2048\n"
    "v_add_u32 v78, 0x1800, v78\n"
    "v_mov_b32 v16, v78\n"
    "s_nop 7\n"
    "s_nop 7\n"
    "v_cmp_eq_u32 vcc, 1, v19\n"
    "v_cndmask_b32 v73, v104, v105, vcc\n"
    "v_cmp_eq_u32 vcc, 2, v19\n"
    "v_cndmask_b32 v73, v73, v106, vcc\n"
    "v_cmp_eq_u32 vcc, 3, v19\n"
    "v_cndmask_b32 v73, v73, v107, vcc\n"
    "v_mul_f32 v72, 0x3ae38e39, v100\n"
    "v_mul_f32 v73, 0x3ae38e39, v73\n"
    "v_fma_f32 v73, -v72, v72, v73\n"
    "v_max_f32 v73, 0, v73\n"
    "v_cvt_pk_bf16_f32 v74, v72, v73\n"
    "v_lshlrev_b32 v72, 16, v74\n"
    "v_and_b32 v73, 0xffff0000, v74\n"
    "v_add_f32 v73, 0x3727c5ac, v73\n"
    "v_cvt_pk_bf16_f32 v73, v73, v73\n"
    "v_lshlrev_b32 v73, 16, v73\n"
    "v_sqrt_f32 v73, v73\n"
    "s_nop 1\n"
    "v_cvt_pk_bf16_f32 v73, v73, v73\n"
    "v_lshlrev_b32 v73, 16, v73\n"
    "v_rcp_f32 v74, v73\n"
    "s_nop 1\n"
    "v_mul_f32 v75, v72, v74\n"
    "v_sub_f32 v75, 0, v75\n"
    "ds_write_b64 v34, v[74:75]\n"
    "s_waitcnt lgkmcnt(0)\n"
    "s_barrier\n"
    "ds_read_b64 v[44:45], v33 offset:0\n"
    "ds_read_b64 v[46:47], v33 offset:128\n"
    "ds_read_b64 v[48:49], v33 offset:256\n"
    "ds_read_b64 v[50:51], v33 offset:384\n"
    "s_mov_b32 s46, 4\n"
    "LP1_%=:\n"
    "s_waitcnt vmcnt(16)\n"
    "v_mfma_f32_16x16x32_bf16 v[136:139], v[152:155], a[0:3], 0\n"
    "ds_read_b128 v[80:83], v32\n"
    "ds_read_b128 v[84:87], v32 offset:2304\n"
    "v_add_u32 v32, 64, v32\n"
    "v_mfma_f32_16x16x32_bf16 v[140:143], v[152:155], a[4:7], 0\n"
    "v_mfma_f32_16x16x32_bf16 v[144:147], v[152:155], a[8:11], 0\n"
    "v_mfma_f32_16x16x32_bf16 v[148:151], v[152:155], a[12:15], 0\n"
    "s_waitcnt lgkmcnt(2)\n"
    "global_load_dwordx4 v[152:155], v16, s[36:37] offset:-3072\n"
    "v_mfma_f32_16x16x32_bf16 v[136:139], v[156:159], a[16:19], v[136:139]\n"
    "v_fma_f32 v52, v45, v36, v40\n"
    "v_fma_f32 v53, v45, v37, v41\n"
    "v_mfma_f32_16x16x32_bf16 v[140:143], v[156:159], a[20:23], v[140:143]\n"
    "v_fma_f32 v54, v45, v38, v42\n"
    "v_fma_f32 v55, v45, v39, v43\n"
    "v_mfma_f32_16x16x32_bf16 v[144:147], v[156:159], a[24:27], v[144:147]\n"
    "v_fma_f32 v52, v120, v44, v52\n"
    "v_mfma_f32_16x16x32_bf16 v[148:151], v[156:159], a[28:31], v[148:151]\n"
    "global_load_dwordx4 v[156:159], v16, s[36:37] offset:-2048\n"
    "s_waitcnt vmcnt(16)\n"
    "v_mfma_f32_16x16x32_bf16 v[136:139], v[160:163], a[32:35], v[136:139]\n"
    "v_fma_f32 v53, v121, v44, v53\n"
    "v_fma_f32 v54, v122, v44, v54\n"
    "v_mfma_f32_16x16x32_bf16 v[140:143], v[160:163], a[36:39], v[140:143]\n"
    "v_fma_f32 v55, v123, v44, v55\n"
    "v_max_f32 v52, 0, v52\n"
    "v_mfma_f32_16x16x32_bf16 v[144:147], v[160:163], a[40:43], v[144:147]\n"
    "v_max_f32 v53, 0, v53\n"
    "v_mfma_f32_16x16x32_bf16 v[148:151], v[160:163], a[44:47], v[148:151]\n"
    "global_load_dwordx4 v[160:163], v16, s[36:37] offset:-1024\n"
    "v_mfma_f32_16x16x32_bf16 v[136:139], v[164:167], a[48:51], v[136:139]\n"
    "v_max_f32 v54, 0, v54\n"
    "v_max_f32 v55, 0, v55\n"
    "v_mfma_f32_16x16x32_bf16 v[140:143], v[164:167], a[52:55], v[140:143]\n"
    "v_mul_f32 v52, v52, v52\n"
    "v_mul_f32 v53, v53, v53\n"
    "v_mfma_f32_16x16x32_bf16 v[144:147], v[164:167], a[56:59], v[144:147]\n"
    "v_mul_f32 v54, v54, v54\n"
    "v_mfma_f32_16x16x32_bf16 v[148:151], v[164:167], a[60:63], v[148:151]\n"
    "global_load_dwordx4 v[164:167], v16, s[36:37] offset:0\n"
    "s_waitcnt vmcnt(16)\n"
    "v_mfma_f32_16x16x32_bf16 v[136:139], v[168:171], a[64:67], v[136:139]\n"
    "v_mul_f32 v55, v55, v55\n"
    "v_cvt_pk_bf16_f32 v56, v52, v53\n"
    "v_mfma_f32_16x16x32_bf16 v[140:143], v[168:171], a[68:71], v[140:143]\n"
    "v_cvt_pk_bf16_f32 v57, v54, v55\n"
    "ds_write_b64 v30, v[56:57] offset:0\n"
    "v_mfma_f32_16x16x32_bf16 v[144:147], v[168:171], a[72:75], v[144:147]\n"
    "v_fma_f32 v52, v47, v36, v40\n"
    "v_mfma_f32_16x16x32_bf16 v[148:151], v[168:171], a[76:79], v[148:151]\n"
    "global_load_dwordx4 v[168:171], v16, s[36:37] offset:1024\n"
    "v_mfma_f32_16x16x32_bf16 v[136:139], v[172:175], a[80:83], v[136:139]\n"
    "v_fma_f32 v53, v47, v37, v41\n"
    "v_fma_f32 v54, v47, v38, v42\n"
    "v_mfma_f32_16x16x32_bf16 v[140:143], v[172:175], a[84:87], v[140:143]\n"
    "v_fma_f32 v55, v47, v39, v43\n"
    "v_fma_f32 v52, v124, v46, v52\n"
    "v_mfma_f32_16x16x32_bf16 v[144:147], v[172:175], a[88:91], v[144:147]\n"
    "v_fma_f32 v53, v125, v46, v53\n"
    "v_mfma_f32_16x16x32_bf16 v[148:151], v[172:175], a[92:95], v[148:151]\n"
    "global_load_dwordx4 v[172:175], v16, s[36:37] offset:2048\n"
    "v_add_u32 v16, 0x1800, v16\n"
    "s_waitcnt vmcnt(16)\n"
    "v_mfma_f32_16x16x32_bf16 v[136:139], v[176:179], a[96:99], v[136:139]\n"
    "v_fma_f32 v54, v126, v46, v54\n"
    "v_fma_f32 v55, v127, v46, v55\n"
    "v_mfma_f32_16x16x32_bf16 v[140:143], v[176:179], a[100:103], v[140:143]\n"
    "v_max_f32 v52, 0, v52\n"
    "v_max_f32 v53, 0, v53\n"
    "v_mfma_f32_16x16x32_bf16 v[144:147], v[176:179], a[104:107], v[144:147]\n"
    "v_max_f32 v54, 0, v54\n"
    "v_mfma_f32_16x16x32_bf16 v[148:151], v[176:179], a[108:111], v[148:151]\n"
    "global_load_dwordx4 v[176:179], v16, s[36:37] offset:-3072\n"
    "v_mfma_f32_16x16x32_bf16 v[136:139], v[180:183], a[112:115], v[136:139]\n"
    "v_max_f32 v55, 0, v55\n"
    "v_mul_f32 v52, v52, v52\n"
    "v_mfma_f32_16x16x32_bf16 v[140:143], v[180:183], a[116:119], v[140:143]\n"
    "v_mul_f32 v53, v53, v53\n"
    "v_mul_f32 v54, v54, v54\n"
    "v_mfma_f32_16x16x32_bf16 v[144:147], v[180:183], a[120:123], v[144:147]\n"
    "v_mul_f32 v55, v55, v55\n"
    "v_mfma_f32_16x16x32_bf16 v[148:151], v[180:183], a[124:127], v[148:151]\n"
    "global_load_dwordx4 v[180:183], v16, s[36:37] offset:-2048\n"
    "s_waitcnt vmcnt(16)\n"
    "v_mfma_f32_16x16x32_bf16 v[136:139], v[184:187], a[128:131], v[136:139]\n"
    "v_cvt_pk_bf16_f32 v58, v52, v53\n"
    "v_cvt_pk_bf16_f32 v59, v54, v55\n"
    "v_mfma_f32_16x16x32_bf16 v[140:143], v[184:187], a[132:135], v[140:143]\n"
    "ds_write_b64 v30, v[58:59] offset:18944\n"
    "v_fma_f32 v52, v49, v36, v40\n"
    "v_mfma_f32_16x16x32_bf16 v[144:147], v[184:187], a[136:139], v[144:147]\n"
    "v_fma_f32 v53, v49, v37, v41\n"
    "v_mfma_f32_16x16x32_bf16 v[148:151], v[184:187], a[140:143], v[148:151]\n"
    "global_load_dwordx4 v[184:187], v16, s[36:37] offset:-1024\n"
    "v_mfma_f32_16x16x32_bf16 v[136:139], v[188:191], a[144:147], v[136:139]\n"
    "v_fma_f32 v54, v49, v38, v42\n"
    "v_fma_f32 v55, v49, v39, v43\n"
    "v_mfma_f32_16x16x32_bf16 v[140:143], v[188:191], a[148:151], v[140:143]\n"
    "v_fma_f32 v52, v128, v48, v52\n"
    "v_fma_f32 v53, v129, v48, v53\n"
    "v_mfma_f32_16x16x32_bf16 v[144:147], v[188:191], a[152:155], v[144:147]\n"
    "v_mfma_f32_16x16x32_bf16 v[148:151], v[188:191], a[156:159], v[148:151]\n"
    "global_load_dwordx4 v[188:191], v16, s[36:37] offset:0\n"
    "s_waitcnt vmcnt(16)\n"
    "v_mfma_f32_16x16x32_bf16 v[136:139], v[192:195], a[160:163], v[136:139]\n"
    "v_fma_f32 v54, v130, v48, v54\n"
    "v_fma_f32 v55, v131, v48, v55\n"
    "v_mfma_f32_16x16x32_bf16 v[140:143], v[192:195], a[164:167], v[140:143]\n"
    "v_max_f32 v52, 0, v52\n"
    "v_max_f32 v53, 0, v53\n"
    "v_mfma_f32_16x16x32_bf16 v[144:147], v[192:195], a[168:171], v[144:147]\n"
    "v_mfma_f32_16x16x32_bf16 v[148:151], v[192:195], a[172:175], v[148:151]\n"
    "global_load_dwordx4 v[192:195], v16, s[36:37] offset:1024\n"
    "v_mfma_f32_16x16x32_bf16 v[136:139], v[196:199], a[176:179], v[136:139]\n"
    "v_max_f32 v54, 0, v54\n"
    "v_max_f32 v55, 0, v55\n"
    "v_mfma_f32_16x16x32_bf16 v[140:143], v[196:199], a[180:183], v[140:143]\n"
    "v_mul_f32 v52, v52, v52\n"
    "v_mul_f32 v53, v53, v53\n"
    "v_mfma_f32_16x16x32_bf16 v[144:147], v[196:199], a[184:187], v[144:147]\n"
    "v_mfma_f32_16x16x32_bf16 v[148:151], v[196:199], a[188:191], v[148:151]\n"
    "global_load_dwordx4 v[196:199], v16, s[36:37] offset:2048\n"
    "v_add_u32 v16, 0x1800, v16\n"
    "s_waitcnt vmcnt(16)\n"
    "v_mfma_f32_16x16x32_bf16 v[136:139], v[200:203], a[192:195], v[136:139]\n"
    "v_mul_f32 v54, v54, v54\n"
    "v_mul_f32 v55, v55, v55\n"
    "v_mfma_f32_16x16x32_bf16 v[140:143], v[200:203], a[196:199], v[140:143]\n"
    "v_cvt_pk_bf16_f32 v56, v52, v53\n"
    "v_cvt_pk_bf16_f32 v57, v54, v55\n"
    "v_mfma_f32_16x16x32_bf16 v[144:147], v[200:203], a[200:203], v[144:147]\n"
    "v_mfma_f32_16x16x32_bf16 v[148:151], v[200:203], a[204:207], v[148:151]\n"
    "global_load_dwordx4 v[200:203], v16, s[36:37] offset:-3072\n"
    "v_mfma_f32_16x16x32_bf16 v[136:139], v[204:207], a[208:211], v[136:139]\n"
    "ds_write_b64 v30, v[56:57] offset:37888\n"
    "v_fma_f32 v52, v51, v36, v40\n"
    "v_mfma_f32_16x16x32_bf16 v[140:143], v[204:207], a[212:215], v[140:143]\n"
    "v_fma_f32 v53, v51, v37, v41\n"
    "v_fma_f32 v54, v51, v38, v42\n"
    "v_mfma_f32_16x16x32_bf16 v[144:147], v[204:207], a[216:219], v[144:147]\n"
    "v_mfma_f32_16x16x32_bf16 v[148:151], v[204:207], a[220:223], v[148:151]\n"
    "global_load_dwordx4 v[204:207], v16, s[36:37] offset:-2048\n"
    "s_waitcnt vmcnt(16)\n"
    "v_mfma_f32_16x16x32_bf16 v[136:139], v[208:211], a[224:227], v[136:139]\n"
    "v_fma_f32 v55, v51, v39, v43\n"
    "v_fma_f32 v52, v132, v50, v52\n"
    "v_mfma_f32_16x16x32_bf16 v[140:143], v[208:211], a[228:231], v[140:143]\n"
    "v_fma_f32 v53, v133, v50, v53\n"
    "v_fma_f32 v54, v134, v50, v54\n"
    "v_mfma_f32_16x16x32_bf16 v[144:147], v[208:211], a[232:235], v[144:147]\n"
    "v_mfma_f32_16x16x32_bf16 v[148:151], v[208:211], a[236:239], v[148:151]\n"
    "global_load_dwordx4 v[208:211], v16, s[36:37] offset:-1024\n"
    "v_mfma_f32_16x16x32_bf16 v[136:139], v[212:215], a[240:243], v[136:139]\n"
    "v_fma_f32 v55, v135, v50, v55\n"
    "v_max_f32 v52, 0, v52\n"
    "v_mfma_f32_16x16x32_bf16 v[140:143], v[212:215], a[244:247], v[140:143]\n"
    "v_max_f32 v53, 0, v53\n"
    "v_max_f32 v54, 0, v54\n"
    "v_mfma_f32_16x16x32_bf16 v[144:147], v[212:215], a[248:251], v[144:147]\n"
    "v_mfma_f32_16x16x32_bf16 v[148:151], v[212:215], a[252:255], v[148:151]\n"
    "global_load_dwordx4 v[212:215], v16, s[36:37] offset:0\n"
    "s_waitcnt vmcnt(16)\n"
    "v_mfma_f32_16x16x32_bf16 v[136:139], v[216:219], v[224:227], v[136:139]\n"
    "v_max_f32 v55, 0, v55\n"
    "v_mul_f32 v52, v52, v52\n"
    "v_mfma_f32_16x16x32_bf16 v[140:143], v[216:219], v[228:231], v[140:143]\n"
    "v_mul_f32 v53, v53, v53\n"
    "v_mul_f32 v54, v54, v54\n"
    "v_mfma_f32_16x16x32_bf16 v[144:147], v[216:219], v[232:235], v[144:147]\n"
    "v_mfma_f32_16x16x32_bf16 v[148:151], v[216:219], v[236:239], v[148:151]\n"
    "global_load_dwordx4 v[216:219], v16, s[36:37] offset:1024\n"
    "v_mfma_f32_16x16x32_bf16 v[136:139], v[220:223], v[240:243], v[136:139]\n"
    "v_mul_f32 v55, v55, v55\n"
    "v_cvt_pk_bf16_f32 v58, v52, v53\n"
    "v_mfma_f32_16x16x32_bf16 v[140:143], v[220:223], v[244:247], v[140:143]\n"
    "v_cvt_pk_bf16_f32 v59, v54, v55\n"
    "ds_write_b64 v30, v[58:59] offset:56832\n"
    "v_mfma_f32_16x16x32_bf16 v[144:147], v[220:223], v[248:251], v[144:147]\n"
    "v_mfma_f32_16x16x32_bf16 v[148:151], v[220:223], v[252:255], v[148:151]\n"
    "global_load_dwordx4 v[220:223], v16, s[36:37] offset:2048\n"
    "v_add_u32 v16, 0x1800, v16\n"
    "s_waitcnt vmcnt(16)\n"
    "v_mfma_f32_16x16x32_bf16 v[120:123], v[152:155], a[0:3], 0\n"
    "ds_read_b128 v[36:39], v32\n"
    "ds_read_b128 v[40:43], v32 offset:2304\n"
    "v_add_u32 v32, 64, v32\n"
    "v_mfma_f32_16x16x32_bf16 v[124:127], v[152:155], a[4:7], 0\n"
    "v_mfma_f32_16x16x32_bf16 v[128:131], v[152:155], a[8:11], 0\n"
    "v_mfma_f32_16x16x32_bf16 v[132:135], v[152:155], a[12:15], 0\n"
    "s_waitcnt lgkmcnt(2)\n"
    "global_load_dwordx4 v[152:155], v16, s[36:37] offset:-3072\n"
    "v_mfma_f32_16x16x32_bf16 v[120:123], v[156:159], a[16:19], v[120:123]\n"
    "v_fma_f32 v52, v45, v80, v84\n"
    "v_fma_f32 v53, v45, v81, v85\n"
    "v_mfma_f32_16x16x32_bf16 v[124:127], v[156:159], a[20:23], v[124:127]\n"
    "v_fma_f32 v54, v45, v82, v86\n"
    "v_fma_f32 v55, v45, v83, v87\n"
    "v_mfma_f32_16x16x32_bf16 v[128:131], v[156:159], a[24:27], v[128:131]\n"
    "v_fma_f32 v52, v136, v44, v52\n"
    "v_mfma_f32_16x16x32_bf16 v[132:135], v[156:159], a[28:31], v[132:135]\n"
    "global_load_dwordx4 v[156:159], v16, s[36:37] offset:-2048\n"
    "s_waitcnt vmcnt(16)\n"
    "v_mfma_f32_16x16x32_bf16 v[120:123], v[160:163], a[32:35], v[120:123]\n"
    "v_fma_f32 v53, v137, v44, v53\n"
    "v_fma_f32 v54, v138, v44, v54\n"
    "v_mfma_f32_16x16x32_bf16 v[124:127], v[160:163], a[36:39], v[124:127]\n"
    "v_fma_f32 v55, v139, v44, v55\n"
    "v_max_f32 v52, 0, v52\n"
    "v_mfma_f32_16x16x32_bf16 v[128:131], v[160:163], a[40:43], v[128:131]\n"
    "v_max_f32 v53, 0, v53\n"
    "v_mfma_f32_16x16x32_bf16 v[132:135], v[160:163], a[44:47], v[132:135]\n"
    "global_load_dwordx4 v[160:163], v16, s[36:37] offset:-1024\n"
    "v_mfma_f32_16x16x32_bf16 v[120:123], v[164:167], a[48:51], v[120:123]\n"
    "v_max_f32 v54, 0, v54\n"
    "v_max_f32 v55, 0, v55\n"
    "v_mfma_f32_16x16x32_bf16 v[124:127], v[164:167], a[52:55], v[124:127]\n"
    "v_mul_f32 v52, v52, v52\n"
    "v_mul_f32 v53, v53, v53\n"
    "v_mfma_f32_16x16x32_bf16 v[128:131], v[164:167], a[56:59], v[128:131]\n"
    "v_mul_f32 v54, v54, v54\n"
    "v_mfma_f32_16x16x32_bf16 v[132:135], v[164:167], a[60:63], v[132:135]\n"
    "global_load_dwordx4 v[164:167], v16, s[36:37] offset:0\n"
    "s_waitcnt vmcnt(16)\n"
    "v_mfma_f32_16x16x32_bf16 v[120:123], v[168:171], a[64:67], v[120:123]\n"
    "v_mul_f32 v55, v55, v55\n"
    "v_cvt_pk_bf16_f32 v56, v52, v53\n"
    "v_mfma_f32_16x16x32_bf16 v[124:127], v[168:171], a[68:71], v[124:127]\n"
    "v_cvt_pk_bf16_f32 v57, v54, v55\n"
    "ds_write_b64 v30, v[56:57] offset:32\n"
    "v_mfma_f32_16x16x32_bf16 v[128:131], v[168:171], a[72:75], v[128:131]\n"
    "v_fma_f32 v52, v47, v80, v84\n"
    "v_mfma_f32_16x16x32_bf16 v[132:135], v[168:171], a[76:79], v[132:135]\n"
    "global_load_dwordx4 v[168:171], v16, s[36:37] offset:1024\n"
    "v_mfma_f32_16x16x32_bf16 v[120:123], v[172:175], a[80:83], v[120:123]\n"
    "v_fma_f32 v53, v47, v81, v85\n"
    "v_fma_f32 v54, v47, v82, v86\n"
    "v_mfma_f32_16x16x32_bf16 v[124:127], v[172:175], a[84:87], v[124:127]\n"
    "v_fma_f32 v55, v47, v83, v87\n"
    "v_fma_f32 v52, v140, v46, v52\n"
    "v_mfma_f32_16x16x32_bf16 v[128:131], v[172:175], a[88:91], v[128:131]\n"
    "v_fma_f32 v53, v141, v46, v53\n"
    "v_mfma_f32_16x16x32_bf16 v[132:135], v[172:175], a[92:95], v[132:135]\n"
    "global_load_dwordx4 v[172:175], v16, s[36:37] offset:2048\n"
    "v_add_u32 v16, 0x1800, v16\n"
    "s_waitcnt vmcnt(16)\n"
    "v_mfma_f32_16x16x32_bf16 v[120:123], v[176:179], a[96:99], v[120:123]\n"
    "v_fma_f32 v54, v142, v46, v54\n"
    "v_fma_f32 v55, v143, v46, v55\n"
    "v_mfma_f32_16x16x32_bf16 v[124:127], v[176:179], a[100:103], v[124:127]\n"
    "v_max_f32 v52, 0, v52\n"
    "v_max_f32 v53, 0, v53\n"
    "v_mfma_f32_16x16x32_bf16 v[128:131], v[176:179], a[104:107], v[128:131]\n"
    "v_max_f32 v54, 0, v54\n"
    "v_mfma_f32_16x16x32_bf16 v[132:135], v[176:179], a[108:111], v[132:135]\n"
    "global_load_dwordx4 v[176:179], v16, s[36:37] offset:-3072\n"
    "v_mfma_f32_16x16x32_bf16 v[120:123], v[180:183], a[112:115], v[120:123]\n"
    "v_max_f32 v55, 0, v55\n"
    "v_mul_f32 v52, v52, v52\n"
    "v_mfma_f32_16x16x32_bf16 v[124:127], v[180:183], a[116:119], v[124:127]\n"
    "v_mul_f32 v53, v53, v53\n"
    "v_mul_f32 v54, v54, v54\n"
    "v_mfma_f32_16x16x32_bf16 v[128:131], v[180:183], a[120:123], v[128:131]\n"
    "v_mul_f32 v55, v55, v55\n"
    "v_mfma_f32_16x16x32_bf16 v[132:135], v[180:183], a[124:127], v[132:135]\n"
    "global_load_dwordx4 v[180:183], v16, s[36:37] offset:-2048\n"
    "s_waitcnt vmcnt(16)\n"
    "v_mfma_f32_16x16x32_bf16 v[120:123], v[184:187], a[128:131], v[120:123]\n"
    "v_cvt_pk_bf16_f32 v58, v52, v53\n"
    "v_cvt_pk_bf16_f32 v59, v54, v55\n"
    "v_mfma_f32_16x16x32_bf16 v[124:127], v[184:187], a[132:135], v[124:127]\n"
    "ds_write_b64 v30, v[58:59] offset:18976\n"
    "v_fma_f32 v52, v49, v80, v84\n"
    "v_mfma_f32_16x16x32_bf16 v[128:131], v[184:187], a[136:139], v[128:131]\n"
    "v_fma_f32 v53, v49, v81, v85\n"
    "v_mfma_f32_16x16x32_bf16 v[132:135], v[184:187], a[140:143], v[132:135]\n"
    "global_load_dwordx4 v[184:187], v16, s[36:37] offset:-1024\n"
    "v_mfma_f32_16x16x32_bf16 v[120:123], v[188:191], a[144:147], v[120:123]\n"
    "v_fma_f32 v54, v49, v82, v86\n"
    "v_fma_f32 v55, v49, v83, v87\n"
    "v_mfma_f32_16x16x32_bf16 v[124:127], v[188:191], a[148:151], v[124:127]\n"
    "v_fma_f32 v52, v144, v48, v52\n"
    "v_fma_f32 v53, v145, v48, v53\n"
    "v_mfma_f32_16x16x32_bf16 v[128:131], v[188:191], a[152:155], v[128:131]\n"
    "v_mfma_f32_16x16x32_bf16 v[132:135], v[188:191], a[156:159], v[132:135]\n"
    "global_load_dwordx4 v[188:191], v16, s[36:37] offset:0\n"
    "s_waitcnt vmcnt(16)\n"
    "v_mfma_f32_16x16x32_bf16 v[120:123], v[192:195], a[160:163], v[120:123]\n"
    "v_fma_f32 v54, v146, v48, v54\n"
    "v_fma_f32 v55, v147, v48, v55\n"
    "v_mfma_f32_16x16x32_bf16 v[124:127], v[192:195], a[164:167], v[124:127]\n"
    "v_max_f32 v52, 0, v52\n"
    "v_max_f32 v53, 0, v53\n"
    "v_mfma_f32_16x16x32_bf16 v[128:131], v[192:195], a[168:171], v[128:131]\n"
    "v_mfma_f32_16x16x32_bf16 v[132:135], v[192:195], a[172:175], v[132:135]\n"
    "global_load_dwordx4 v[192:195], v16, s[36:37] offset:1024\n"
    "v_mfma_f32_16x16x32_bf16 v[120:123], v[196:199], a[176:179], v[120:123]\n"
    "v_max_f32 v54, 0, v54\n"
    "v_max_f32 v55, 0, v55\n"
    "v_mfma_f32_16x16x32_bf16 v[124:127], v[196:199], a[180:183], v[124:127]\n"
    "v_mul_f32 v52, v52, v52\n"
    "v_mul_f32 v53, v53, v53\n"
    "v_mfma_f32_16x16x32_bf16 v[128:131], v[196:199], a[184:187], v[128:131]\n"
    "v_mfma_f32_16x16x32_bf16 v[132:135], v[196:199], a[188:191], v[132:135]\n"
    "global_load_dwordx4 v[196:199], v16, s[36:37] offset:2048\n"
    "v_add_u32 v16, 0x1800, v16\n"
    "s_waitcnt vmcnt(16)\n"
    "v_mfma_f32_16x16x32_bf16 v[120:123], v[200:203], a[192:195], v[120:123]\n"
    "v_mul_f32 v54, v54, v54\n"
    "v_mul_f32 v55, v55, v55\n"
    "v_mfma_f32_16x16x32_bf16 v[124:127], v[200:203], a[196:199], v[124:127]\n"
    "v_cvt_pk_bf16_f32 v56, v52, v53\n"
    "v_cvt_pk_bf16_f32 v57, v54, v55\n"
    "v_mfma_f32_16x16x32_bf16 v[128:131], v[200:203], a[200:203], v[128:131]\n"
    "v_mfma_f32_16x16x32_bf16 v[132:135], v[200:203], a[204:207], v[132:135]\n"
    "global_load_dwordx4 v[200:203], v16, s[36:37] offset:-3072\n"
    "v_mfma_f32_16x16x32_bf16 v[120:123], v[204:207], a[208:211], v[120:123]\n"
    "ds_write_b64 v30, v[56:57] offset:37920\n"
    "v_fma_f32 v52, v51, v80, v84\n"
    "v_mfma_f32_16x16x32_bf16 v[124:127], v[204:207], a[212:215], v[124:127]\n"
    "v_fma_f32 v53, v51, v81, v85\n"
    "v_fma_f32 v54, v51, v82, v86\n"
    "v_mfma_f32_16x16x32_bf16 v[128:131], v[204:207], a[216:219], v[128:131]\n"
    "v_mfma_f32_16x16x32_bf16 v[132:135], v[204:207], a[220:223], v[132:135]\n"
    "global_load_dwordx4 v[204:207], v16, s[36:37] offset:-2048\n"
    "s_waitcnt vmcnt(16)\n"
    "v_mfma_f32_16x16x32_bf16 v[120:123], v[208:211], a[224:227], v[120:123]\n"
    "v_fma_f32 v55, v51, v83, v87\n"
    "v_fma_f32 v52, v148, v50, v52\n"
    "v_mfma_f32_16x16x32_bf16 v[124:127], v[208:211], a[228:231], v[124:127]\n"
    "v_fma_f32 v53, v149, v50, v53\n"
    "v_fma_f32 v54, v150, v50, v54\n"
    "v_mfma_f32_16x16x32_bf16 v[128:131], v[208:211], a[232:235], v[128:131]\n"
    "v_mfma_f32_16x16x32_bf16 v[132:135], v[208:211], a[236:239], v[132:135]\n"
    "global_load_dwordx4 v[208:211], v16, s[36:37] offset:-1024\n"
    "v_mfma_f32_16x16x32_bf16 v[120:123], v[212:215], a[240:243], v[120:123]\n"
    "v_fma_f32 v55, v151, v50, v55\n"
    "v_max_f32 v52, 0, v52\n"
    "v_mfma_f32_16x16x32_bf16 v[124:127], v[212:215], a[244:247], v[124:127]\n"
    "v_max_f32 v53, 0, v53\n"
    "v_max_f32 v54, 0, v54\n"
    "v_mfma_f32_16x16x32_bf16 v[128:131], v[212:215], a[248:251], v[128:131]\n"
    "v_mfma_f32_16x16x32_bf16 v[132:135], v[212:215], a[252:255], v[132:135]\n"
    "global_load_dwordx4 v[212:215], v16, s[36:37] offset:0\n"
    "s_waitcnt vmcnt(16)\n"
    "v_mfma_f32_16x16x32_bf16 v[120:123], v[216:219], v[224:227], v[120:123]\n"
    "v_max_f32 v55, 0, v55\n"
    "v_mul_f32 v52, v52, v52\n"
    "v_mfma_f32_16x16x32_bf16 v[124:127], v[216:219], v[228:231], v[124:127]\n"
    "v_mul_f32 v53, v53, v53\n"
    "v_mul_f32 v54, v54, v54\n"
    "v_mfma_f32_16x16x32_bf16 v[128:131], v[216:219], v[232:235], v[128:131]\n"
    "v_mfma_f32_16x16x32_bf16 v[132:135], v[216:219], v[236:239], v[132:135]\n"
    "global_load_dwordx4 v[216:219], v16, s[36:37] offset:1024\n"
    "v_mfma_f32_16x16x32_bf16 v[120:123], v[220:223], v[240:243], v[120:123]\n"
    "v_mul_f32 v55, v55, v55\n"
    "v_cvt_pk_bf16_f32 v58, v52, v53\n"
    "v_mfma_f32_16x16x32_bf16 v[124:127], v[220:223], v[244:247], v[124:127]\n"
    "v_cvt_pk_bf16_f32 v59, v54, v55\n"
    "ds_write_b64 v30, v[58:59] offset:56864\n"
    "v_mfma_f32_16x16x32_bf16 v[128:131], v[220:223], v[248:251], v[128:131]\n"
    "v_mfma_f32_16x16x32_bf16 v[132:135], v[220:223], v[252:255], v[132:135]\n"
    "global_load_dwordx4 v[220:223], v16, s[36:37] offset:2048\n"
    "v_add_u32 v16, 0x1800, v16\n"
    "v_add_u32 v30, 64, v30\n"
    "s_sub_u32 s46, s46, 1\n"
    "s_cmp_lg_u32 s46, 0\n"
    "s_cbranch_scc1 LP1_%=\n"
    "v_mov_b32 v20, -1\n"
    "v_mov_b32 v21, -1\n"
    "v_mov_b32 v22, -1\n"
    "v_mov_b32 v23, -1\n"
    "s_sub_u32 s61, %[ss], 6\n"
    "s_and_b32 s61, s61, 7\n"
    "s_mul_i32 s61, s61, 9216\n"
    "s_mov_b64 s[62:63], %[sown]\n"
    "s_add_u32 s62, s62, s61\n"
    "s_addc_u32 s63, s63, 0\n"
    "global_store_dwordx4 v17, v[20:23], s[62:63] sc1\n"
    "global_store_dwordx4 v17, v[20:23], s[62:63] offset:1024 sc1\n"
    "s_sub_u32 s61, %[ss], 7\n"
    "s_and_b32 s61, s61, 7\n"
    "s_mul_i32 s61, s61, 9216\n"
    "s_mov_b64 s[62:63], %[sown]\n"
    "s_add_u32 s62, s62, s61\n"
    "s_addc_u32 s63, s63, 0\n"
    "global_store_dwordx4 v17, v[20:23], s[62:63] sc1\n"
    "global_store_dwordx4 v17, v[20:23], s[62:63] offset:1024 sc1\n"
    "s_waitcnt lgkmcnt(0)\n"
    "s_nop 7\n"
    "v_fma_f32 v52, v45, v36, v40\n"
    "v_fma_f32 v53, v45, v37, v41\n"
    "v_fma_f32 v54, v45, v38, v42\n"
    "v_fma_f32 v55, v45, v39, v43\n"
    "v_fma_f32 v52, v120, v44, v52\n"
    "v_fma_f32 v53, v121, v44, v53\n"
    "v_fma_f32 v54, v122, v44, v54\n"
    "v_fma_f32 v55, v123, v44, v55\n"
    "v_max_f32 v52, 0, v52\n"
    "v_max_f32 v53, 0, v53\n"
    "v_max_f32 v54, 0, v54\n"
    "v_max_f32 v55, 0, v55\n"
    "v_mul_f32 v52, v52, v52\n"
    "v_mul_f32 v53, v53, v53\n"
    "v_mul_f32 v54, v54, v54\n"
    "v_mul_f32 v55, v55, v55\n"
    "v_cvt_pk_bf16_f32 v56, v52, v53\n"
    "v_cvt_pk_bf16_f32 v57, v54, v55\n"
    "ds_write_b64 v30, v[56:57] offset:0\n"
    "v_fma_f32 v52, v47, v36, v40\n"
    "v_fma_f32 v53, v47, v37, v41\n"
    "v_fma_f32 v54, v47, v38, v42\n"
    "v_fma_f32 v55, v47, v39, v43\n"
    "v_fma_f32 v52, v124, v46, v52\n"
    "v_fma_f32 v53, v125, v46, v53\n"
    "v_fma_f32 v54, v126, v46, v54\n"
    "v_fma_f32 v55, v127, v46, v55\n"
    "v_max_f32 v52, 0, v52\n"
    "v_max_f32 v53, 0, v53\n"
    "v_max_f32 v54, 0, v54\n"
    "v_max_f32 v55, 0, v55\n"
    "v_mul_f32 v52, v52, v52\n"
    "v_mul_f32 v53, v53, v53\n"
    "v_mul_f32 v54, v54, v54\n"
    "v_mul_f32 v55, v55, v55\n"
    "v_cvt_pk_bf16_f32 v58, v52, v53\n"
    "v_cvt_pk_bf16_f32 v59, v54, v55\n"
    "ds_write_b64 v30, v[58:59] offset:18944\n"
    "v_fma_f32 v52, v49, v36, v40\n"
    "v_fma_f32 v53, v49, v37, v41\n"
    "v_fma_f32 v54, v49, v38, v42\n"
    "v_fma_f32 v55, v49, v39, v43\n"
    "v_fma_f32 v52, v128, v48, v52\n"
    "v_fma_f32 v53, v129, v48, v53\n"
    "v_fma_f32 v54, v130, v48, v54\n"
    "v_fma_f32 v55, v131, v48, v55\n"
    "v_max_f32 v52, 0, v52\n"
    "v_max_f32 v53, 0, v53\n"
    "v_max_f32 v54, 0, v54\n"
    "v_max_f32 v55, 0, v55\n"
    "v_mul_f32 v52, v52, v52\n"
    "v_mul_f32 v53, v53, v53\n"
    "v_mul_f32 v54, v54, v54\n"
    "v_mul_f32 v55, v55, v55\n"
    "v_cvt_pk_bf16_f32 v56, v52, v53\n"
    "v_cvt_pk_bf16_f32 v57, v54, v55\n"
    "ds_write_b64 v30, v[56:57] offset:37888\n"
    "v_fma_f32 v52, v51, v36, v40\n"
    "v_fma_f32 v53, v51, v37, v41\n"
    "v_fma_f32 v54, v51, v38, v42\n"
    "v_fma_f32 v55, v51, v39, v43\n"
    "v_fma_f32 v52, v132, v50, v52\n"
    "v_fma_f32 v53, v133, v50, v53\n"
    "v_fma_f32 v54, v134, v50, v54\n"
    "v_fma_f32 v55, v135, v50, v55\n"
    "v_max_f32 v52, 0, v52\n"
    "v_max_f32 v53, 0, v53\n"
    "v_max_f32 v54, 0, v54\n"
    "v_max_f32 v55, 0, v55\n"
    "v_mul_f32 v52, v52, v52\n"
    "v_mul_f32 v53, v53, v53\n"
    "v_mul_f32 v54, v54, v54\n"
    "v_mul_f32 v55, v55, v55\n"
    "v_cvt_pk_bf16_f32 v58, v52, v53\n"
    "v_cvt_pk_bf16_f32 v59, v54, v55\n"
    "ds_write_b64 v30, v[58:59] offset:56832\n"
    "s_waitcnt lgkmcnt(0)\n"
    "s_barrier\n"
    "ds_read_b128 a[0:3], v31 offset:0\n"
    "ds_read_b128 a[4:7], v31 offset:18944\n"
    "ds_read_b128 a[8:11], v31 offset:37888\n"
    "ds_read_b128 a[12:15], v31 offset:56832\n"
    "ds_read_b128 a[16:19], v31 offset:64\n"
    "ds_read_b128 a[20:23], v31 offset:19008\n"
    "ds_read_b128 a[24:27], v31 offset:37952\n"
    "ds_read_b128 a[28:31], v31 offset:56896\n"
    "s_waitcnt vmcnt(16)\n"
    "s_waitcnt lgkmcnt(4)\n"
    "v_mfma_f32_16x16x32_bf16 v[120:123], v[152:155], a[0:3], 0\n"
    "ds_read_b128 a[32:35], v31 offset:128\n"
    "ds_read_b128 a[36:39], v31 offset:19072\n"
    "ds_read_b128 a[40:43], v31 offset:38016\n"
    "ds_read_b128 a[44:47], v31 offset:56960\n"
    "v_mfma_f32_16x16x32_bf16 v[124:127], v[152:155], a[4:7], 0\n"
    "v_mfma_f32_16x16x32_bf16 v[128:131], v[152:155], a[8:11], 0\n"
    "v_mfma_f32_16x16x32_bf16 v[132:135], v[152:155], a[12:15], 0\n"
    "global_load_dwordx4 v[152:155], v16, s[36:37] offset:-3072\n"
    "s_waitcnt lgkmcnt(4)\n"
    "v_mfma_f32_16x16x32_bf16 v[120:123], v[156:159], a[16:19], v[120:123]\n"
    "ds_read_b128 a[48:51], v31 offset:192\n"
    "ds_read_b128 a[52:55], v31 offset:19136\n"
    "ds_read_b128 a[56:59], v31 offset:38080\n"
    "ds_read_b128 a[60:63], v31 offset:57024\n"
    "v_mfma_f32_16x16x32_bf16 v[124:127], v[156:159], a[20:23], v[124:127]\n"
    "v_mfma_f32_16x16x32_bf16 v[128:131], v[156:159], a[24:27], v[128:131]\n"
    "v_mfma_f32_16x16x32_bf16 v[132:135], v[156:159], a[28:31], v[132:135]\n"
    "global_load_dwordx4 v[156:159], v16, s[36:37] offset:-2048\n"
    "s_waitcnt vmcnt(16)\n"
    "s_waitcnt lgkmcnt(4)\n"
    "v_mfma_f32_16x16x32_bf16 v[120:123], v[160:163], a[32:35], v[120:123]\n"
    "ds_read_b128 a[64:67], v31 offset:256\n"
    "ds_read_b128 a[68:71], v31 offset:19200\n"
    "ds_read_b128 a[72:75], v31 offset:38144\n"
    "ds_read_b128 a[76:79], v31 offset:57088\n"
    "v_mfma_f32_16x16x32_bf16 v[124:127], v[160:163], a[36:39], v[124:127]\n"
    "v_mfma_f32_16x16x32_bf16 v[128:131], v[160:163], a[40:43], v[128:131]\n"
    "v_mfma_f32_16x16x32_bf16 v[132:135], v[160:163], a[44:47], v[132:135]\n"
    "global_load_dwordx4 v[160:163], v16, s[36:37] offset:-1024\n"
    "s_waitcnt lgkmcnt(4)\n"
    "v_mfma_f32_16x16x32_bf16 v[120:123], v[164:167], a[48:51], v[120:123]\n"
    "ds_read_b128 a[80:83], v31 offset:320\n"
    "ds_read_b128 a[84:87], v31 offset:19264\n"
    "ds_read_b128 a[88:91], v31 offset:38208\n"
    "ds_read_b128 a[92:95], v31 offset:57152\n"
    "v_mfma_f32_16x16x32_bf16 v[124:127], v[164:167], a[52:55], v[124:127]\n"
    "v_mfma_f32_16x16x32_bf16 v[128:131], v[164:167], a[56:59], v[128:131]\n"
    "v_mfma_f32_16x16x32_bf16 v[132:135], v[164:167], a[60:63], v[132:135]\n"
    "global_load_dwordx4 v[164:167], v16, s[36:37] offset:0\n"
    "s_waitcnt vmcnt(16)\n"
    "s_waitcnt lgkmcnt(4)\n"
    "v_mfma_f32_16x16x32_bf16 v[120:123], v[168:171], a[64:67], v[120:123]\n"
    "ds_read_b128 a[96:99], v31 offset:384\n"
    "ds_read_b128 a[100:103], v31 offset:19328\n"
    "ds_read_b128 a[104:107], v31 offset:38272\n"
    "ds_read_b128 a[108:111], v31 offset:57216\n"
    "v_mfma_f32_16x16x32_bf16 v[124:127], v[168:171], a[68:71], v[124:127]\n"
    "v_mfma_f32_16x16x32_bf16 v[128:131], v[168:171], a[72:75], v[128:131]\n"
    "v_mfma_f32_16x16x32_bf16 v[132:135], v[168:171], a[76:79], v[132:135]\n"
    "global_load_dwordx4 v[168:171], v16, s[36:37] offset:1024\n"
    "s_waitcnt lgkmcnt(4)\n"
    "v_mfma_f32_16x16x32_bf16 v[120:123], v[172:175], a[80:83], v[120:123]\n"
    "ds_read_b128 a[112:115], v31 offset:448\n"
    "ds_read_b128 a[116:119], v31 offset:19392\n"
    "ds_read_b128 a[120:123], v31 offset:38336\n"
    "ds_read_b128 a[124:127], v31 offset:57280\n"
    "v_mfma_f32_16x16x32_bf16 v[124:127], v[172:175], a[84:87], v[124:127]\n"
    "v_mfma_f32_16x16x32_bf16 v[128:131], v[172:175], a[88:91], v[128:131]\n"
    "v_mfma_f32_16x16x32_bf16 v[132:135], v[172:175], a[92:95], v[132:135]\n"
    "global_load_dwordx4 v[172:175], v16, s[36:37] offset:2048\n"
    "v_add_u32 v16, 0x1800, v16\n"
    "s_waitcnt vmcnt(16)\n"
    "s_waitcnt lgkmcnt(4)\n"
    "v_mfma_f32_16x16x32_bf16 v[120:123], v[176:179], a[96:99], v[120:123]\n"
    "ds_read_b128 a[128:131], v31 offset:512\n"
    "ds_read_b128 a[132:135], v31 offset:19456\n"
    "ds_read_b128 a[136:139], v31 offset:38400\n"
    "ds_read_b128 a[140:143], v31 offset:57344\n"
    "v_mfma_f32_16x16x32_bf16 v[124:127], v[176:179], a[100:103], v[124:127]\n"
    "v_mfma_f32_16x16x32_bf16 v[128:131], v[176:179], a[104:107], v[128:131]\n"
    "v_mfma_f32_16x16x32_bf16 v[132:135], v[176:179], a[108:111], v[132:135]\n"
    "global_load_dwordx4 v[176:179], v16, s[36:37] offset:-3072\n"
    "s_waitcnt lgkmcnt(4)\n"
    "v_mfma_f32_16x16x32_bf16 v[120:123], v[180:183], a[112:115], v[120:123]\n"
    "ds_read_b128 a[144:147], v31 offset:576\n"
    "ds_read_b128 a[148:151], v31 offset:19520\n"
    "ds_read_b128 a[152:155], v31 offset:38464\n"
    "ds_read_b128 a[156:159], v31 offset:57408\n"
    "v_mfma_f32_16x16x32_bf16 v[124:127], v[180:183], a[116:119], v[124:127]\n"
    "v_mfma_f32_16x16x32_bf16 v[128:131], v[180:183], a[120:123], v[128:131]\n"
    "v_mfma_f32_16x16x32_bf16 v[132:135], v[180:183], a[124:127], v[132:135]\n"
    "global_load_dwordx4 v[180:183], v16, s[36:37] offset:-2048\n"
    "s_waitcnt vmcnt(16)\n"
    "s_waitcnt lgkmcnt(4)\n"
    "v_mfma_f32_16x16x32_bf16 v[120:123], v[184:187], a[128:131], v[120:123]\n"
    "ds_read_b128 a[160:163], v31 offset:640\n"
    "ds_read_b128 a[164:167], v31 offset:19584\n"
    "ds_read_b128 a[168:171], v31 offset:38528\n"
    "ds_read_b128 a[172:175], v31 offset:57472\n"
    "v_mfma_f32_16x16x32_bf16 v[124:127], v[184:187], a[132:135], v[124:127]\n"
    "v_mfma_f32_16x16x32_bf16 v[128:131], v[184:187], a[136:139], v[128:131]\n"
    "v_mfma_f32_16x16x32_bf16 v[132:135], v[184:187], a[140:143], v[132:135]\n"
    "global_load_dwordx4 v[184:187], v16, s[36:37] offset:-1024\n"
    "s_waitcnt lgkmcnt(4)\n"
    "v_mfma_f32_16x16x32_bf16 v[120:123], v[188:191], a[144:147], v[120:123]\n"
    "ds_read_b128 a[176:179], v31 offset:704\n"
    "ds_read_b128 a[180:183], v31 offset:19648\n"
    "ds_read_b128 a[184:187], v31 offset:38592\n"
    "ds_read_b128 a[188:191], v31 offset:57536\n"
    "v_mfma_f32_16x16x32_bf16 v[124:127], v[188:191], a[148:151], v[124:127]\n"
    "v_mfma_f32_16x16x32_bf16 v[128:131], v[188:191], a[152:155], v[128:131]\n"
    "v_mfma_f32_16x16x32_bf16 v[132:135], v[188:191], a[156:159], v[132:135]\n"
    "global_load_dwordx4 v[188:191], v16, s[36:37] offset:0\n"
    "s_waitcnt vmcnt(16)\n"
    "s_waitcnt lgkmcnt(4)\n"
    "v_mfma_f32_16x16x32_bf16 v[120:123], v[192:195], a[160:163], v[120:123]\n"
    "ds_read_b128 a[192:195], v31 offset:768\n"
    "ds_read_b128 a[196:199], v31 offset:19712\n"
    "ds_read_b128 a[200:203], v31 offset:38656\n"
    "ds_read_b128 a[204:207], v31 offset:57600\n"
    "v_mfma_f32_16x16x32_bf16 v[124:127], v[192:195], a[164:167], v[124:127]\n"
    "v_mfma_f32_16x16x32_bf16 v[128:131], v[192:195], a[168:171], v[128:131]\n"
    "v_mfma_f32_16x16x32_bf16 v[132:135], v[192:195], a[172:175], v[132:135]\n"
    "global_load_dwordx4 v[192:195], v16, s[36:37] offset:1024\n"
    "s_waitcnt lgkmcnt(4)\n"
    "v_mfma_f32_16x16x32_bf16 v[120:123], v[196:199], a[176:179], v[120:123]\n"
    "ds_read_b128 a[208:211], v31 offset:832\n"
    "ds_read_b128 a[212:215], v31 offset:19776\n"
    "ds_read_b128 a[216:219], v31 offset:38720\n"
    "ds_read_b128 a[220:223], v31 offset:57664\n"
    "v_mfma_f32_16x16x32_bf16 v[124:127], v[196:199], a[180:183], v[124:127]\n"
    "v_mfma_f32_16x16x32_bf16 v[128:131], v[196:199], a[184:187], v[128:131]\n"
    "v_mfma_f32_16x16x32_bf16 v[132:135], v[196:199], a[188:191], v[132:135]\n"
    "global_load_dwordx4 v[196:199], v16, s[36:37] offset:2048\n"
    "v_add_u32 v16, 0x1800, v16\n"
    "s_waitcnt vmcnt(16)\n"
    "s_waitcnt lgkmcnt(4)\n"
    "v_mfma_f32_16x16x32_bf16 v[120:123], v[200:203], a[192:195], v[120:123]\n"
    "ds_read_b128 a[224:227], v31 offset:896\n"
    "ds_read_b128 a[228:231], v31 offset:19840\n"
    "ds_read_b128 a[232:235], v31 offset:38784\n"
    "ds_read_b128 a[236:239], v31 offset:57728\n"
    "v_mfma_f32_16x16x32_bf16 v[124:127], v[200:203], a[196:199], v[124:127]\n"
    "v_mfma_f32_16x16x32_bf16 v[128:131], v[200:203], a[200:203], v[128:131]\n"
    "v_mfma_f32_16x16x32_bf16 v[132:135], v[200:203], a[204:207], v[132:135]\n"
    "global_load_dwordx4 v[200:203], v16, s[36:37] offset:-3072\n"
    "s_waitcnt lgkmcnt(4)\n"
    "v_mfma_f32_16x16x32_bf16 v[120:123], v[204:207], a[208:211], v[120:123]\n"
    "ds_read_b128 a[240:243], v31 offset:960\n"
    "ds_read_b128 a[244:247], v31 offset:19904\n"
    "ds_read_b128 a[248:251], v31 offset:38848\n"
    "ds_read_b128 a[252:255], v31 offset:57792\n"
    "v_mfma_f32_16x16x32_bf16 v[124:127], v[204:207], a[212:215], v[124:127]\n"
    "v_mfma_f32_16x16x32_bf16 v[128:131], v[204:207], a[216:219], v[128:131]\n"
    "v_mfma_f32_16x16x32_bf16 v[132:135], v[204:207], a[220:223], v[132:135]\n"
    "global_load_dwordx4 v[204:207], v16, s[36:37] offset:-2048\n"
    "s_waitcnt vmcnt(16)\n"
    "s_waitcnt lgkmcnt(4)\n"
    "v_mfma_f32_16x16x32_bf16 v[120:123], v[208:211], a[224:227], v[120:123]\n"
    "ds_read_b128 v[224:227], v31 offset:1024\n"
    "ds_read_b128 v[228:231], v31 offset:19968\n"
    "ds_read_b128 v[232:235], v31 offset:38912\n"
    "ds_read_b128 v[236:239], v31 offset:57856\n"
    "v_mfma_f32_16x16x32_bf16 v[124:127], v[208:211], a[228:231], v[124:127]\n"
    "v_mfma_f32_16x16x32_bf16 v[128:131], v[208:211], a[232:235], v[128:131]\n"
    "v_mfma_f32_16x16x32_bf16 v[132:135], v[208:211], a[236:239], v[132:135]\n"
    "global_load_dwordx4 v[208:211], v16, s[36:37] offset:-1024\n"
    "s_waitcnt lgkmcnt(4)\n"
    "v_mfma_f32_16x16x32_bf16 v[120:123], v[212:215], a[240:243], v[120:123]\n"
    "ds_read_b128 v[240:243], v31 offset:1088\n"
    "ds_read_b128 v[244:247], v31 offset:20032\n"
    "ds_read_b128 v[248:251], v31 offset:38976\n"
    "ds_read_b128 v[252:255], v31 offset:57920\n"
    "v_mfma_f32_16x16x32_bf16 v[124:127], v[212:215], a[244:247], v[124:127]\n"
    "v_mfma_f32_16x16x32_bf16 v[128:131], v[212:215], a[248:251], v[128:131]\n"
    "v_mfma_f32_16x16x32_bf16 v[132:135], v[212:215], a[252:255], v[132:135]\n"
    "global_load_dwordx4 v[212:215], v16, s[36:37] offset:0\n"
    "s_waitcnt vmcnt(16)\n"
    "s_waitcnt lgkmcnt(4)\n"
    "v_mfma_f32_16x16x32_bf16 v[120:123], v[216:219], v[224:227], v[120:123]\n"
    "v_mfma_f32_16x16x32_bf16 v[124:127], v[216:219], v[228:231], v[124:127]\n"
    "v_mfma_f32_16x16x32_bf16 v[128:131], v[216:219], v[232:235], v[128:131]\n"
    "v_mfma_f32_16x16x32_bf16 v[132:135], v[216:219], v[236:239], v[132:135]\n"
    "global_load_dwordx4 v[216:219], v16, s[36:37] offset:1024\n"
    "s_waitcnt lgkmcnt(0)\n"
    "v_mfma_f32_16x16x32_bf16 v[120:123], v[220:223], v[240:243], v[120:123]\n"
    "v_mfma_f32_16x16x32_bf16 v[124:127], v[220:223], v[244:247], v[124:127]\n"
    "v_mfma_f32_16x16x32_bf16 v[128:131], v[220:223], v[248:251], v[128:131]\n"
    "v_mfma_f32_16x16x32_bf16 v[132:135], v[220:223], v[252:255], v[132:135]\n"
    "global_load_dwordx4 v[220:223], v16, s[36:37] offset:2048\n"
    "v_add_u32 v16, 0x1800, v16\n"
    "s_waitcnt vmcnt(16)\n"
    "v_mfma_f32_16x16x32_bf16 v[136:139], v[152:155], a[0:3], 0\n"
    "v_mfma_f32_16x16x32_bf16 v[140:143], v[152:155], a[4:7], 0\n"
    "v_mfma_f32_16x16x32_bf16 v[144:147], v[152:155], a[8:11], 0\n"
    "v_mfma_f32_16x16x32_bf16 v[148:151], v[152:155], a[12:15], 0\n"
    "global_load_dwordx4 v[152:155], v16, s[36:37] offset:-3072\n"
    "v_mfma_f32_16x16x32_bf16 v[136:139], v[156:159], a[16:19], v[136:139]\n"
    "v_cvt_pk_bf16_f32 v56, v120, v121\n"
    "v_cvt_pk_bf16_f32 v57, v122, v123\n"
    "v_mfma_f32_16x16x32_bf16 v[140:143], v[156:159], a[20:23], v[140:143]\n"
    "v_cvt_pk_bf16_f32 v58, v124, v125\n"
    "v_cvt_pk_bf16_f32 v59, v126, v127\n"
    "v_mfma_f32_16x16x32_bf16 v[144:147], v[156:159], a[24:27], v[144:147]\n"
    "global_store_dwordx4 v17, v[56:59], s[40:41] offset:0 sc1\n"
    "v_mfma_f32_16x16x32_bf16 v[148:151], v[156:159], a[28:31], v[148:151]\n"
    "global_load_dwordx4 v[156:159], v16, s[36:37] offset:-2048\n"
    "s_waitcnt vmcnt(16)\n"
    "v_mfma_f32_16x16x32_bf16 v[136:139], v[160:163], a[32:35], v[136:139]\n"
    "v_cvt_pk_bf16_f32 v60, v128, v129\n"
    "v_cvt_pk_bf16_f32 v61, v130, v131\n"
    "v_mfma_f32_16x16x32_bf16 v[140:143], v[160:163], a[36:39], v[140:143]\n"
    "v_cvt_pk_bf16_f32 v62, v132, v133\n"
    "v_cvt_pk_bf16_f32 v63, v134, v135\n"
    "v_mfma_f32_16x16x32_bf16 v[144:147], v[160:163], a[40:43], v[144:147]\n"
    "global_store_dwordx4 v17, v[60:63], s[42:43] offset:0 sc1\n"
    "v_mfma_f32_16x16x32_bf16 v[148:151], v[160:163], a[44:47], v[148:151]\n"
    "global_load_dwordx4 v[160:163], v16, s[36:37] offset:-1024\n"
    "v_mfma_f32_16x16x32_bf16 v[136:139], v[164:167], a[48:51], v[136:139]\n"
    "s_mov_b64 s[48:49], %[sbase]\n"
    "s_mov_b32 s50, %[srot]\n"
    "v_mfma_f32_16x16x32_bf16 v[140:143], v[164:167], a[52:55], v[140:143]\n"
    "s_mov_b64 s[54:55], %[sown]\n"
    "s_mov_b64 s[56:57], %[sout]\n"
    "v_mfma_f32_16x16x32_bf16 v[144:147], v[164:167], a[56:59], v[144:147]\n"
    "s_mov_b64 s[58:59], %[sb2]\n"
    "v_mfma_f32_16x16x32_bf16 v[148:151], v[164:167], a[60:63], v[148:151]\n"
    "global_load_dwordx4 v[164:167], v16, s[36:37] offset:0\n"
    "s_waitcnt vmcnt(16)\n"
    "v_mfma_f32_16x16x32_bf16 v[136:139], v[168:171], a[64:67], v[136:139]\n"
    "s_mov_b64 s[64:65], %[sfe]\n"
    "s_mov_b32 s60, %[swv]\n"
    "v_mfma_f32_16x16x32_bf16 v[140:143], v[168:171], a[68:71], v[140:143]\n"
    "s_lshl_b32 s52, s60, 7\n"
    "s_add_u32 s51, %[ss], 1\n"
    "v_mfma_f32_16x16x32_bf16 v[144:147], v[168:171], a[72:75], v[144:147]\n"
    "s_and_b32 s51, s51, 7\n"
    "v_mfma_f32_16x16x32_bf16 v[148:151], v[168:171], a[76:79], v[148:151]\n"
    "global_load_dwordx4 v[168:171], v16, s[36:37] offset:1024\n"
    "v_mfma_f32_16x16x32_bf16 v[136:139], v[172:175], a[80:83], v[136:139]\n"
    "s_lshl_b32 s51, s51, 9\n"
    "s_cmp_eq_u32 s60, 0\n"
    "v_mfma_f32_16x16x32_bf16 v[140:143], v[172:175], a[84:87], v[140:143]\n"
    "s_cselect_b32 s53, 8, 0\n"
    "v_mov_b32 v24, 0\n"
    "v_mfma_f32_16x16x32_bf16 v[144:147], v[172:175], a[88:91], v[144:147]\n"
    "v_mov_b32 v26, 1\n"
    "v_mfma_f32_16x16x32_bf16 v[148:151], v[172:175], a[92:95], v[148:151]\n"
    "global_load_dwordx4 v[172:175], v16, s[36:37] offset:2048\n"
    "v_add_u32 v16, 0x1800, v16\n"
    "s_waitcnt vmcnt(16)\n"
    "v_mfma_f32_16x16x32_bf16 v[136:139], v[176:179], a[96:99], v[136:139]\n"
    "s_lshl_b32 s47, s60, 10\n"
    "s_add_u32 s47, s47, 8\n"
    "v_mfma_f32_16x16x32_bf16 v[140:143], v[176:179], a[100:103], v[140:143]\n"
    "v_lshlrev_b32 v25, 5, v17\n"
    "v_add_u32 v25, s47, v25\n"
    "v_mfma_f32_16x16x32_bf16 v[144:147], v[176:179], a[104:107], v[144:147]\n"
    "v_add_u32 v27, %[sdx], %[vb2]\n"
    "v_mfma_f32_16x16x32_bf16 v[148:151], v[176:179], a[108:111], v[148:151]\n"
    "global_load_dwordx4 v[176:179], v16, s[36:37] offset:-3072\n"
    "v_mfma_f32_16x16x32_bf16 v[136:139], v[180:183], a[112:115], v[136:139]\n"
    "s_mul_i32 s47, %[shx], 576\n"
    "s_add_u32 s47, s47, %[sdx]\n"
    "v_mfma_f32_16x16x32_bf16 v[140:143], v[180:183], a[116:119], v[140:143]\n"
    "v_add_u32 v19, s47, %[vout]\n"
    "s_lshl_b32 s47, %[sdx], 6\n"
    "v_mfma_f32_16x16x32_bf16 v[144:147], v[180:183], a[120:123], v[144:147]\n"
    "v_xor_b32 v18, %[shx], %[vho]\n"
    "v_mfma_f32_16x16x32_bf16 v[148:151], v[180:183], a[124:127], v[148:151]\n"
    "global_load_dwordx4 v[180:183], v16, s[36:37] offset:-2048\n"
    "s_waitcnt vmcnt(16)\n"
    "v_mfma_f32_16x16x32_bf16 v[136:139], v[184:187], a[128:131], v[136:139]\n"
    "v_add_u32 v18, s47, v18\n"
    "v_mov_b32 v32, %[vsx]\n"
    "v_mfma_f32_16x16x32_bf16 v[140:143], v[184:187], a[132:135], v[140:143]\n"
    "v_mov_b32 v33, %[vsy]\n"
    "v_mov_b32 v34, 0\n"
    "v_mfma_f32_16x16x32_bf16 v[144:147], v[184:187], a[136:139], v[144:147]\n"
    "v_mov_b32 v35, 0\n"
    "v_mfma_f32_16x16x32_bf16 v[148:151], v[184:187], a[140:143], v[148:151]\n"
    "global_load_dwordx4 v[184:187], v16, s[36:37] offset:-1024\n"
    "v_mfma_f32_16x16x32_bf16 v[136:139], v[188:191], a[144:147], v[136:139]\n"
    "v_mov_b32 v36, 0\n"
    "v_mov_b32 v37, 0\n"
    "v_mfma_f32_16x16x32_bf16 v[140:143], v[188:191], a[148:151], v[140:143]\n"
    "v_mov_b32 v38, %[vsx]\n"
    "v_mov_b32 v39, %[vsy]\n"
    "v_mfma_f32_16x16x32_bf16 v[144:147], v[188:191], a[152:155], v[144:147]\n"
    "v_mfma_f32_16x16x32_bf16 v[148:151], v[188:191], a[156:159], v[148:151]\n"
    "global_load_dwordx4 v[188:191], v16, s[36:37] offset:0\n"
    "s_waitcnt vmcnt(16)\n"
    "v_mfma_f32_16x16x32_bf16 v[136:139], v[192:195], a[160:163], v[136:139]\n"
    "v_mfma_f32_16x16x32_bf16 v[140:143], v[192:195], a[164:167], v[140:143]\n"
    "v_mfma_f32_16x16x32_bf16 v[144:147], v[192:195], a[168:171], v[144:147]\n"
    "v_mfma_f32_16x16x32_bf16 v[148:151], v[192:195], a[172:175], v[148:151]\n"
    "global_load_dwordx4 v[192:195], v16, s[36:37] offset:1024\n"
    "v_mfma_f32_16x16x32_bf16 v[136:139], v[196:199], a[176:179], v[136:139]\n"
    "v_mfma_f32_16x16x32_bf16 v[140:143], v[196:199], a[180:183], v[140:143]\n"
    "v_mfma_f32_16x16x32_bf16 v[144:147], v[196:199], a[184:187], v[144:147]\n"
    "v_mfma_f32_16x16x32_bf16 v[148:151], v[196:199], a[188:191], v[148:151]\n"
    "global_load_dwordx4 v[196:199], v16, s[36:37] offset:2048\n"
    "v_add_u32 v16, 0x1800, v16\n"
    "s_waitcnt vmcnt(16)\n"
    "v_mfma_f32_16x16x32_bf16 v[136:139], v[200:203], a[192:195], v[136:139]\n"
    "v_mfma_f32_16x16x32_bf16 v[140:143], v[200:203], a[196:199], v[140:143]\n"
    "v_mfma_f32_16x16x32_bf16 v[144:147], v[200:203], a[200:203], v[144:147]\n"
    "v_mfma_f32_16x16x32_bf16 v[148:151], v[200:203], a[204:207], v[148:151]\n"
    "global_load_dwordx4 v[200:203], v16, s[36:37] offset:-3072\n"
    "v_mfma_f32_16x16x32_bf16 v[136:139], v[204:207], a[208:211], v[136:139]\n"
    "v_mfma_f32_16x16x32_bf16 v[140:143], v[204:207], a[212:215], v[140:143]\n"
    "v_mfma_f32_16x16x32_bf16 v[144:147], v[204:207], a[216:219], v[144:147]\n"
    "v_mfma_f32_16x16x32_bf16 v[148:151], v[204:207], a[220:223], v[148:151]\n"
    "global_load_dwordx4 v[204:207], v16, s[36:37] offset:-2048\n"
    "s_waitcnt vmcnt(16)\n"
    "v_mfma_f32_16x16x32_bf16 v[136:139], v[208:211], a[224:227], v[136:139]\n"
    "v_mfma_f32_16x16x32_bf16 v[140:143], v[208:211], a[228:231], v[140:143]\n"
    "v_mfma_f32_16x16x32_bf16 v[144:147], v[208:211], a[232:235], v[144:147]\n"
    "v_mfma_f32_16x16x32_bf16 v[148:151], v[208:211], a[236:239], v[148:151]\n"
    "global_load_dwordx4 v[208:211], v16, s[36:37] offset:-1024\n"
    "v_mfma_f32_16x16x32_bf16 v[136:139], v[212:215], a[240:243], v[136:139]\n"
    "v_mfma_f32_16x16x32_bf16 v[140:143], v[212:215], a[244:247], v[140:143]\n"
    "v_mfma_f32_16x16x32_bf16 v[144:147], v[212:215], a[248:251], v[144:147]\n"
    "v_mfma_f32_16x16x32_bf16 v[148:151], v[212:215], a[252:255], v[148:151]\n"
    "global_load_dwordx4 v[212:215], v16, s[36:37] offset:0\n"
    "s_waitcnt vmcnt(16)\n"
    "v_mfma_f32_16x16x32_bf16 v[136:139], v[216:219], v[224:227], v[136:139]\n"
    "v_mfma_f32_16x16x32_bf16 v[140:143], v[216:219], v[228:231], v[140:143]\n"
    "v_mfma_f32_16x16x32_bf16 v[144:147], v[216:219], v[232:235], v[144:147]\n"
    "v_mfma_f32_16x16x32_bf16 v[148:151], v[216:219], v[236:239], v[148:151]\n"
    "global_load_dwordx4 v[216:219], v16, s[36:37] offset:1024\n"
    "v_mfma_f32_16x16x32_bf16 v[136:139], v[220:223], v[240:243], v[136:139]\n"
    "v_mfma_f32_16x16x32_bf16 v[140:143], v[220:223], v[244:247], v[140:143]\n"
    "v_mfma_f32_16x16x32_bf16 v[144:147], v[220:223], v[248:251], v[144:147]\n"
    "v_mfma_f32_16x16x32_bf16 v[148:151], v[220:223], v[252:255], v[148:151]\n"
    "global_load_dwordx4 v[220:223], v16, s[36:37] offset:2048\n"
    "v_add_u32 v16, 0x1800, v16\n"
    "s_waitcnt vmcnt(16)\n"
    "v_mfma_f32_16x16x32_bf16 v[120:123], v[152:155], a[0:3], 0\n"
    "v_mfma_f32_16x16x32_bf16 v[124:127], v[152:155], a[4:7], 0\n"
    "v_mfma_f32_16x16x32_bf16 v[128:131], v[152:155], a[8:11], 0\n"
    "v_mfma_f32_16x16x32_bf16 v[132:135], v[152:155], a[12:15], 0\n"
    "global_load_dwordx4 v[152:155], v16, s[36:37] offset:-3072\n"
    "v_mfma_f32_16x16x32_bf16 v[120:123], v[156:159], a[16:19], v[120:123]\n"
    "s_add_u32 s40, s48, s50\n"
    "s_addc_u32 s41, s49, 0\n"
    "v_mfma_f32_16x16x32_bf16 v[124:127], v[156:159], a[20:23], v[124:127]\n"
    "s_add_u32 s50, s50, 0x12000\n"
    "s_cmp_eq_u32 s50, 0x90000\n"
    "v_mfma_f32_16x16x32_bf16 v[128:131], v[156:159], a[24:27], v[128:131]\n"
    "s_cselect_b32 s50, 0, s50\n"
    "v_mfma_f32_16x16x32_bf16 v[132:135], v[156:159], a[28:31], v[132:135]\n"
    "global_load_dwordx4 v[156:159], v16, s[36:37] offset:-2048\n"
    "s_waitcnt vmcnt(16)\n"
    "v_mfma_f32_16x16x32_bf16 v[120:123], v[160:163], a[32:35], v[120:123]\n"
    "v_cvt_pk_bf16_f32 v56, v136, v137\n"
    "v_cvt_pk_bf16_f32 v57, v138, v139\n"
    "v_mfma_f32_16x16x32_bf16 v[124:127], v[160:163], a[36:39], v[124:127]\n"
    "v_cvt_pk_bf16_f32 v58, v140, v141\n"
    "v_cvt_pk_bf16_f32 v59, v142, v143\n"
    "v_mfma_f32_16x16x32_bf16 v[128:131], v[160:163], a[40:43], v[128:131]\n"
    "global_store_dwordx4 v17, v[56:59], s[40:41] offset:0 sc1\n"
    "v_mfma_f32_16x16x32_bf16 v[132:135], v[160:163], a[44:47], v[132:135]\n"
    "global_load_dwordx4 v[160:163], v16, s[36:37] offset:-1024\n"
    "v_mfma_f32_16x16x32_bf16 v[120:123], v[164:167], a[48:51], v[120:123]\n"
    "v_cvt_pk_bf16_f32 v60, v144, v145\n"
    "v_cvt_pk_bf16_f32 v61, v146, v147\n"
    "v_mfma_f32_16x16x32_bf16 v[124:127], v[164:167], a[52:55], v[124:127]\n"
    "v_cvt_pk_bf16_f32 v62, v148, v149\n"
    "v_cvt_pk_bf16_f32 v63, v150, v151\n"
    "v_mfma_f32_16x16x32_bf16 v[128:131], v[164:167], a[56:59], v[128:131]\n"
    "global_store_dwordx4 v17, v[60:63], s[40:41] offset:1024 sc1\n"
    "v_mfma_f32_16x16x32_bf16 v[132:135], v[164:167], a[60:63], v[132:135]\n"
    "global_load_dwordx4 v[164:167], v16, s[36:37] offset:0\n"
    "s_waitcnt vmcnt(16)\n"
    "v_mfma_f32_16x16x32_bf16 v[120:123], v[168:171], a[64:67], v[120:123]\n"
    "v_mfma_f32_16x16x32_bf16 v[124:127], v[168:171], a[68:71], v[124:127]\n"
    "s_mov_b64 exec, 3\n"
    "global_atomic_add v25, v26, s[44:45]\n"
    "s_mov_b64 exec, -1\n"
    "v_mfma_f32_16x16x32_bf16 v[128:131], v[168:171], a[72:75], v[128:131]\n"
    "v_mfma_f32_16x16x32_bf16 v[132:135], v[168:171], a[76:79], v[132:135]\n"
    "global_load_dwordx4 v[168:171], v16, s[36:37] offset:1024\n"
    "v_mfma_f32_16x16x32_bf16 v[120:123], v[172:175], a[80:83], v[120:123]\n"
    "v_mfma_f32_16x16x32_bf16 v[124:127], v[172:175], a[84:87], v[124:127]\n"
    "v_mfma_f32_16x16x32_bf16 v[128:131], v[172:175], a[88:91], v[128:131]\n"
    "v_mfma_f32_16x16x32_bf16 v[132:135], v[172:175], a[92:95], v[132:135]\n"
    "global_load_dwordx4 v[172:175], v16, s[36:37] offset:2048\n"
    "v_add_u32 v16, 0x1800, v16\n"
    "s_waitcnt vmcnt(16)\n"
    "v_mfma_f32_16x16x32_bf16 v[120:123], v[176:179], a[96:99], v[120:123]\n"
    "v_lshrrev_b32 v21, 4, v17\n"
    "s_add_u32 s47, %[ss], 1\n"
    "v_add_u32 v20, s47, v21\n"
    "v_and_b32 v20, 7, v20\n"
    "v_lshlrev_b32 v20, 9, v20\n"
    "v_add_u32 v21, 1, v21\n"
    "v_add_u32 v20, s52, v20\n"
    "v_lshlrev_b32 v21, 8, v21\n"
    "s_mov_b64 exec, 3\n"
    "global_atomic_add v20, v21, s[44:45]\n"
    "s_mov_b64 exec, -1\n"
    "v_mfma_f32_16x16x32_bf16 v[124:127], v[176:179], a[100:103], v[124:127]\n"
    "v_mfma_f32_16x16x32_bf16 v[128:131], v[176:179], a[104:107], v[128:131]\n"
    "v_mfma_f32_16x16x32_bf16 v[132:135], v[176:179], a[108:111], v[132:135]\n"
    "global_load_dwordx4 v[176:179], v16, s[36:37] offset:-3072\n"
    "v_mfma_f32_16x16x32_bf16 v[120:123], v[180:183], a[112:115], v[120:123]\n"
    "v_mfma_f32_16x16x32_bf16 v[124:127], v[180:183], a[116:119], v[124:127]\n"
    "v_mfma_f32_16x16x32_bf16 v[128:131], v[180:183], a[120:123], v[128:131]\n"
    "v_mfma_f32_16x16x32_bf16 v[132:135], v[180:183], a[124:127], v[132:135]\n"
    "global_load_dwordx4 v[180:183], v16, s[36:37] offset:-2048\n"
    "s_waitcnt vmcnt(16)\n"
    "v_mfma_f32_16x16x32_bf16 v[120:123], v[184:187], a[128:131], v[120:123]\n"
    "v_mfma_f32_16x16x32_bf16 v[124:127], v[184:187], a[132:135], v[124:127]\n"
    "v_mfma_f32_16x16x32_bf16 v[128:131], v[184:187], a[136:139], v[128:131]\n"
    "v_mfma_f32_16x16x32_bf16 v[132:135], v[184:187], a[140:143], v[132:135]\n"
    "global_load_dwordx4 v[184:187], v16, s[36:37] offset:-1024\n"
    "v_mfma_f32_16x16x32_bf16 v[120:123], v[188:191], a[144:147], v[120:123]\n"
    "v_mfma_f32_16x16x32_bf16 v[124:127], v[188:191], a[148:151], v[124:127]\n"
    "v_mfma_f32_16x16x32_bf16 v[128:131], v[188:191], a[152:155], v[128:131]\n"
    "v_mfma_f32_16x16x32_bf16 v[132:135], v[188:191], a[156:159], v[132:135]\n"
    "global_load_dwordx4 v[188:191], v16, s[36:37] offset:0\n"
    "s_waitcnt vmcnt(16)\n"
    "v_mfma_f32_16x16x32_bf16 v[120:123], v[192:195], a[160:163], v[120:123]\n"
    "v_mfma_f32_16x16x32_bf16 v[124:127], v[192:195], a[164:167], v[124:127]\n"
    "v_mfma_f32_16x16x32_bf16 v[128:131], v[192:195], a[168:171], v[128:131]\n"
    "v_mfma_f32_16x16x32_bf16 v[132:135], v[192:195], a[172:175], v[132:135]\n"
    "global_load_dwordx4 v[192:195], v16, s[36:37] offset:1024\n"
    "v_mfma_f32_16x16x32_bf16 v[120:123], v[196:199], a[176:179], v[120:123]\n"
    "v_mfma_f32_16x16x32_bf16 v[124:127], v[196:199], a[180:183], v[124:127]\n"
    "v_mfma_f32_16x16x32_bf16 v[128:131], v[196:199], a[184:187], v[128:131]\n"
    "v_mfma_f32_16x16x32_bf16 v[132:135], v[196:199], a[188:191], v[132:135]\n"
    "global_load_dwordx4 v[196:199], v16, s[36:37] offset:2048\n"
    "v_add_u32 v16, 0x1800, v16\n"
    "s_waitcnt vmcnt(16)\n"
    "v_mfma_f32_16x16x32_bf16 v[120:123], v[200:203], a[192:195], v[120:123]\n"
    "v_mfma_f32_16x16x32_bf16 v[124:127], v[200:203], a[196:199], v[124:127]\n"
    "v_mfma_f32_16x16x32_bf16 v[128:131], v[200:203], a[200:203], v[128:131]\n"
    "v_mfma_f32_16x16x32_bf16 v[132:135], v[200:203], a[204:207], v[132:135]\n"
    "global_load_dwordx4 v[200:203], v16, s[36:37] offset:-3072\n"
    "v_mfma_f32_16x16x32_bf16 v[120:123], v[204:207], a[208:211], v[120:123]\n"
    "v_mfma_f32_16x16x32_bf16 v[124:127], v[204:207], a[212:215], v[124:127]\n"
    "v_mfma_f32_16x16x32_bf16 v[128:131], v[204:207], a[216:219], v[128:131]\n"
    "v_mfma_f32_16x16x32_bf16 v[132:135], v[204:207], a[220:223], v[132:135]\n"
    "global_load_dwordx4 v[204:207], v16, s[36:37] offset:-2048\n"
    "s_waitcnt vmcnt(16)\n"
    "v_mfma_f32_16x16x32_bf16 v[120:123], v[208:211], a[224:227], v[120:123]\n"
    "v_mfma_f32_16x16x32_bf16 v[124:127], v[208:211], a[228:231], v[124:127]\n"
    "v_mfma_f32_16x16x32_bf16 v[128:131], v[208:211], a[232:235], v[128:131]\n"
    "v_mfma_f32_16x16x32_bf16 v[132:135], v[208:211], a[236:239], v[132:135]\n"
    "global_load_dwordx4 v[208:211], v16, s[36:37] offset:-1024\n"
    "v_mfma_f32_16x16x32_bf16 v[120:123], v[212:215], a[240:243], v[120:123]\n"
    "v_mfma_f32_16x16x32_bf16 v[124:127], v[212:215], a[244:247], v[124:127]\n"
    "v_mfma_f32_16x16x32_bf16 v[128:131], v[212:215], a[248:251], v[128:131]\n"
    "v_mfma_f32_16x16x32_bf16 v[132:135], v[212:215], a[252:255], v[132:135]\n"
    "global_load_dwordx4 v[212:215], v16, s[36:37] offset:0\n"
    "s_waitcnt vmcnt(16)\n"
    "v_mfma_f32_16x16x32_bf16 v[120:123], v[216:219], v[224:227], v[120:123]\n"
    "v_mfma_f32_16x16x32_bf16 v[124:127], v[216:219], v[228:231], v[124:127]\n"
    "v_mfma_f32_16x16x32_bf16 v[128:131], v[216:219], v[232:235], v[128:131]\n"
    "v_mfma_f32_16x16x32_bf16 v[132:135], v[216:219], v[236:239], v[132:135]\n"
    "global_load_dwordx4 v[216:219], v16, s[36:37] offset:1024\n"
    "v_mfma_f32_16x16x32_bf16 v[120:123], v[220:223], v[240:243], v[120:123]\n"
    "v_mfma_f32_16x16x32_bf16 v[124:127], v[220:223], v[244:247], v[124:127]\n"
    "v_mfma_f32_16x16x32_bf16 v[128:131], v[220:223], v[248:251], v[128:131]\n"
    "v_mfma_f32_16x16x32_bf16 v[132:135], v[220:223], v[252:255], v[132:135]\n"
    "global_load_dwordx4 v[220:223], v16, s[36:37] offset:2048\n"
    "v_add_u32 v16, 0x1800, v16\n"
    "s_waitcnt vmcnt(16)\n"
    "v_mfma_f32_16x16x32_bf16 v[136:139], v[152:155], a[0:3], 0\n"
    "v_mfma_f32_16x16x32_bf16 v[140:143], v[152:155], a[4:7], 0\n"
    "v_mfma_f32_16x16x32_bf16 v[144:147], v[152:155], a[8:11], 0\n"
    "v_mfma_f32_16x16x32_bf16 v[148:151], v[152:155], a[12:15], 0\n"
    "global_load_dwordx4 v[152:155], v16, s[36:37] offset:-3072\n"
    "v_mfma_f32_16x16x32_bf16 v[136:139], v[156:159], a[16:19], v[136:139]\n"
    "s_add_u32 s40, s48, s50\n"
    "s_addc_u32 s41, s49, 0\n"
    "v_mfma_f32_16x16x32_bf16 v[140:143], v[156:159], a[20:23], v[140:143]\n"
    "s_add_u32 s50, s50, 0x12000\n"
    "s_cmp_eq_u32 s50, 0x90000\n"
    "v_mfma_f32_16x16x32_bf16 v[144:147], v[156:159], a[24:27], v[144:147]\n"
    "s_cselect_b32 s50, 0, s50\n"
    "v_mfma_f32_16x16x32_bf16 v[148:151], v[156:159], a[28:31], v[148:151]\n"
    "global_load_dwordx4 v[156:159], v16, s[36:37] offset:-2048\n"
    "s_waitcnt vmcnt(16)\n"
    "v_mfma_f32_16x16x32_bf16 v[136:139], v[160:163], a[32:35], v[136:139]\n"
    "v_cvt_pk_bf16_f32 v56, v120, v121\n"
    "v_cvt_pk_bf16_f32 v57, v122, v123\n"
    "v_mfma_f32_16x16x32_bf16 v[140:143], v[160:163], a[36:39], v[140:143]\n"
    "v_cvt_pk_bf16_f32 v58, v124, v125\n"
    "v_cvt_pk_bf16_f32 v59, v126, v127\n"
    "v_mfma_f32_16x16x32_bf16 v[144:147], v[160:163], a[40:43], v[144:147]\n"
    "global_store_dwordx4 v17, v[56:59], s[40:41] offset:0 sc1\n"
    "v_mfma_f32_16x16x32_bf16 v[148:151], v[160:163], a[44:47], v[148:151]\n"
    "global_load_dwordx4 v[160:163], v16, s[36:37] offset:-1024\n"
    "v_mfma_f32_16x16x32_bf16 v[136:139], v[164:167], a[48:51], v[136:139]\n"
    "v_cvt_pk_bf16_f32 v60, v128, v129\n"
    "v_cvt_pk_bf16_f32 v61, v130, v131\n"
    "v_mfma_f32_16x16x32_bf16 v[140:143], v[164:167], a[52:55], v[140:143]\n"
    "v_cvt_pk_bf16_f32 v62, v132, v133\n"
    "v_cvt_pk_bf16_f32 v63, v134, v135\n"
    "v_mfma_f32_16x16x32_bf16 v[144:147], v[164:167], a[56:59], v[144:147]\n"
    "global_store_dwordx4 v17, v[60:63], s[40:41] offset:1024 sc1\n"
    "v_mfma_f32_16x16x32_bf16 v[148:151], v[164:167], a[60:63], v[148:151]\n"
    "global_load_dwordx4 v[164:167], v16, s[36:37] offset:0\n"
    "s_waitcnt vmcnt(16)\n"
    "v_mfma_f32_16x16x32_bf16 v[136:139], v[168:171], a[64:67], v[136:139]\n"
    "v_mfma_f32_16x16x32_bf16 v[140:143], v[168:171], a[68:71], v[140:143]\n"
    "s_add_u32 s47, s51, s52\n"
    "s_add_u32 s51, s51, 512\n"
    "s_and_b32 s51, s51, 0xfff\n"
    "v_mov_b32 v25, s47\n"
    "v_mov_b32 v26, 2\n"
    "s_mov_b64 exec, 1\n"
    "global_atomic_add v25, v26, s[44:45]\n"
    "s_mov_b64 exec, -1\n"
    "v_mfma_f32_16x16x32_bf16 v[144:147], v[168:171], a[72:75], v[144:147]\n"
    "v_mfma_f32_16x16x32_bf16 v[148:151], v[168:171], a[76:79], v[148:151]\n"
    "global_load_dwordx4 v[168:171], v16, s[36:37] offset:1024\n"
    "v_mfma_f32_16x16x32_bf16 v[136:139], v[172:175], a[80:83], v[136:139]\n"
    "v_mfma_f32_16x16x32_bf16 v[140:143], v[172:175], a[84:87], v[140:143]\n"
    "v_mfma_f32_16x16x32_bf16 v[144:147], v[172:175], a[88:91], v[144:147]\n"
    "v_mfma_f32_16x16x32_bf16 v[148:151], v[172:175], a[92:95], v[148:151]\n"
    "global_load_dwordx4 v[172:175], v16, s[36:37] offset:2048\n"
    "v_add_u32 v16, 0x1800, v16\n"
    "s_waitcnt vmcnt(16)\n"
    "v_mfma_f32_16x16x32_bf16 v[136:139], v[176:179], a[96:99], v[136:139]\n"
    "global_load_dwordx2 v[40:41], %[vb2], s[58:59]\n"
    "global_load_dwordx2 v[42:43], v27, s[58:59]\n"
    "v_mfma_f32_16x16x32_bf16 v[140:143], v[176:179], a[100:103], v[140:143]\n"
    "v_mfma_f32_16x16x32_bf16 v[144:147], v[176:179], a[104:107], v[144:147]\n"
    "v_mfma_f32_16x16x32_bf16 v[148:151], v[176:179], a[108:111], v[148:151]\n"
    "global_load_dwordx4 v[176:179], v16, s[36:37] offset:-3072\n"
    "v_mfma_f32_16x16x32_bf16 v[136:139], v[180:183], a[112:115], v[136:139]\n"
    "v_mfma_f32_16x16x32_bf16 v[140:143], v[180:183], a[116:119], v[140:143]\n"
    "v_mfma_f32_16x16x32_bf16 v[144:147], v[180:183], a[120:123], v[144:147]\n"
    "v_mfma_f32_16x16x32_bf16 v[148:151], v[180:183], a[124:127], v[148:151]\n"
    "global_load_dwordx4 v[180:183], v16, s[36:37] offset:-2048\n"
    "s_waitcnt vmcnt(16)\n"
    "v_mfma_f32_16x16x32_bf16 v[136:139], v[184:187], a[128:131], v[136:139]\n"
    "v_mfma_f32_16x16x32_bf16 v[140:143], v[184:187], a[132:135], v[140:143]\n"
    "v_mfma_f32_16x16x32_bf16 v[144:147], v[184:187], a[136:139], v[144:147]\n"
    "v_mfma_f32_16x16x32_bf16 v[148:151], v[184:187], a[140:143], v[148:151]\n"
    "global_load_dwordx4 v[184:187], v16, s[36:37] offset:-1024\n"
    "v_mfma_f32_16x16x32_bf16 v[136:139], v[188:191], a[144:147], v[136:139]\n"
    "v_mfma_f32_16x16x32_bf16 v[140:143], v[188:191], a[148:151], v[140:143]\n"
    "v_mfma_f32_16x16x32_bf16 v[144:147], v[188:191], a[152:155], v[144:147]\n"
    "v_mfma_f32_16x16x32_bf16 v[148:151], v[188:191], a[156:159], v[148:151]\n"
    "global_load_dwordx4 v[188:191], v16, s[36:37] offset:0\n"
    "s_waitcnt vmcnt(16)\n"
    "v_mfma_f32_16x16x32_bf16 v[136:139], v[192:195], a[160:163], v[136:139]\n"
    "v_mfma_f32_16x16x32_bf16 v[140:143], v[192:195], a[164:167], v[140:143]\n"
    "v_mfma_f32_16x16x32_bf16 v[144:147], v[192:195], a[168:171], v[144:147]\n"
    "v_mfma_f32_16x16x32_bf16 v[148:151], v[192:195], a[172:175], v[148:151]\n"
    "global_load_dwordx4 v[192:195], v16, s[36:37] offset:1024\n"
    "v_mfma_f32_16x16x32_bf16 v[136:139], v[196:199], a[176:179], v[136:139]\n"
    "v_mfma_f32_16x16x32_bf16 v[140:143], v[196:199], a[180:183], v[140:143]\n"
    "v_mfma_f32_16x16x32_bf16 v[144:147], v[196:199], a[184:187], v[144:147]\n"
    "v_mfma_f32_16x16x32_bf16 v[148:151], v[196:199], a[188:191], v[148:151]\n"
    "global_load_dwordx4 v[196:199], v16, s[36:37] offset:2048\n"
    "v_add_u32 v16, 0x1800, v16\n"
    "s_waitcnt vmcnt(16)\n"
    "v_mfma_f32_16x16x32_bf16 v[136:139], v[200:203], a[192:195], v[136:139]\n"
    "v_mfma_f32_16x16x32_bf16 v[140:143], v[200:203], a[196:199], v[140:143]\n"
    "v_mfma_f32_16x16x32_bf16 v[144:147], v[200:203], a[200:203], v[144:147]\n"
    "v_mfma_f32_16x16x32_bf16 v[148:151], v[200:203], a[204:207], v[148:151]\n"
    "global_load_dwordx4 v[200:203], v16, s[36:37] offset:-3072\n"
    "v_mfma_f32_16x16x32_bf16 v[136:139], v[204:207], a[208:211], v[136:139]\n"
    "v_mfma_f32_16x16x32_bf16 v[140:143], v[204:207], a[212:215], v[140:143]\n"
    "v_mfma_f32_16x16x32_bf16 v[144:147], v[204:207], a[216:219], v[144:147]\n"
    "v_mfma_f32_16x16x32_bf16 v[148:151], v[204:207], a[220:223], v[148:151]\n"
    "global_load_dwordx4 v[204:207], v16, s[36:37] offset:-2048\n"
    "s_waitcnt vmcnt(16)\n"
    "v_mfma_f32_16x16x32_bf16 v[136:139], v[208:211], a[224:227], v[136:139]\n"
    "v_mfma_f32_16x16x32_bf16 v[140:143], v[208:211], a[228:231], v[140:143]\n"
    "v_mfma_f32_16x16x32_bf16 v[144:147], v[208:211], a[232:235], v[144:147]\n"
    "global_load_dwordx4 v[20:23], v24, s[64:65] sc1\n"
    "v_mfma_f32_16x16x32_bf16 v[148:151], v[208:211], a[236:239], v[148:151]\n"
    "global_load_dwordx4 v[208:211], v16, s[36:37] offset:-1024\n"
    "v_mfma_f32_16x16x32_bf16 v[136:139], v[212:215], a[240:243], v[136:139]\n"
    "v_mfma_f32_16x16x32_bf16 v[140:143], v[212:215], a[244:247], v[140:143]\n"
    "v_mfma_f32_16x16x32_bf16 v[144:147], v[212:215], a[248:251], v[144:147]\n"
    "v_mfma_f32_16x16x32_bf16 v[148:151], v[212:215], a[252:255], v[148:151]\n"
    "global_load_dwordx4 v[212:215], v16, s[36:37] offset:0\n"
    "s_waitcnt vmcnt(16)\n"
    "v_mfma_f32_16x16x32_bf16 v[136:139], v[216:219], v[224:227], v[136:139]\n"
    "v_mfma_f32_16x16x32_bf16 v[140:143], v[216:219], v[228:231], v[140:143]\n"
    "v_mfma_f32_16x16x32_bf16 v[144:147], v[216:219], v[232:235], v[144:147]\n"
    "v_mfma_f32_16x16x32_bf16 v[148:151], v[216:219], v[236:239], v[148:151]\n"
    "global_load_dwordx4 v[216:219], v16, s[36:37] offset:1024\n"
    "v_mfma_f32_16x16x32_bf16 v[136:139], v[220:223], v[240:243], v[136:139]\n"
    "v_mfma_f32_16x16x32_bf16 v[140:143], v[220:223], v[244:247], v[140:143]\n"
    "v_mfma_f32_16x16x32_bf16 v[144:147], v[220:223], v[248:251], v[144:147]\n"
    "v_mfma_f32_16x16x32_bf16 v[148:151], v[220:223], v[252:255], v[148:151]\n"
    "global_load_dwordx4 v[220:223], v16, s[36:37] offset:2048\n"
    "v_add_u32 v16, 0x1800, v16\n"
    "s_waitcnt vmcnt(16)\n"
    "v_mfma_f32_16x16x32_bf16 v[120:123], v[152:155], a[0:3], 0\n"
    "v_mfma_f32_16x16x32_bf16 v[124:127], v[152:155], a[4:7], 0\n"
    "v_mfma_f32_16x16x32_bf16 v[128:131], v[152:155], a[8:11], 0\n"
    "v_mfma_f32_16x16x32_bf16 v[132:135], v[152:155], a[12:15], 0\n"
    "global_load_dwordx4 v[152:155], v16, s[36:37] offset:-3072\n"
    "v_mfma_f32_16x16x32_bf16 v[120:123], v[156:159], a[16:19], v[120:123]\n"
    "s_add_u32 s40, s48, s50\n"
    "s_addc_u32 s41, s49, 0\n"
    "v_mfma_f32_16x16x32_bf16 v[124:127], v[156:159], a[20:23], v[124:127]\n"
    "s_add_u32 s50, s50, 0x12000\n"
    "s_cmp_eq_u32 s50, 0x90000\n"
    "v_mfma_f32_16x16x32_bf16 v[128:131], v[156:159], a[24:27], v[128:131]\n"
    "s_cselect_b32 s50, 0, s50\n"
    "v_mfma_f32_16x16x32_bf16 v[132:135], v[156:159], a[28:31], v[132:135]\n"
    "global_load_dwordx4 v[156:159], v16, s[36:37] offset:-2048\n"
    "s_waitcnt vmcnt(16)\n"
    "v_mfma_f32_16x16x32_bf16 v[120:123], v[160:163], a[32:35], v[120:123]\n"
    "v_cvt_pk_bf16_f32 v56, v136, v137\n"
    "v_cvt_pk_bf16_f32 v57, v138, v139\n"
    "v_mfma_f32_16x16x32_bf16 v[124:127], v[160:163], a[36:39], v[124:127]\n"
    "v_cvt_pk_bf16_f32 v58, v140, v141\n"
    "v_cvt_pk_bf16_f32 v59, v142, v143\n"
    "v_mfma_f32_16x16x32_bf16 v[128:131], v[160:163], a[40:43], v[128:131]\n"
    "global_store_dwordx4 v17, v[56:59], s[40:41] offset:0 sc1\n"
    "v_mfma_f32_16x16x32_bf16 v[132:135], v[160:163], a[44:47], v[132:135]\n"
    "global_load_dwordx4 v[160:163], v16, s[36:37] offset:-1024\n"
    "v_mfma_f32_16x16x32_bf16 v[120:123], v[164:167], a[48:51], v[120:123]\n"
    "v_cvt_pk_bf16_f32 v60, v144, v145\n"
    "v_cvt_pk_bf16_f32 v61, v146, v147\n"
    "v_mfma_f32_16x16x32_bf16 v[124:127], v[164:167], a[52:55], v[124:127]\n"
    "v_cvt_pk_bf16_f32 v62, v148, v149\n"
    "v_cvt_pk_bf16_f32 v63, v150, v151\n"
    "v_mfma_f32_16x16x32_bf16 v[128:131], v[164:167], a[56:59], v[128:131]\n"
    "global_store_dwordx4 v17, v[60:63], s[40:41] offset:1024 sc1\n"
    "v_mfma_f32_16x16x32_bf16 v[132:135], v[164:167], a[60:63], v[132:135]\n"
    "global_load_dwordx4 v[164:167], v16, s[36:37] offset:0\n"
    "s_waitcnt vmcnt(16)\n"
    "v_mfma_f32_16x16x32_bf16 v[120:123], v[168:171], a[64:67], v[120:123]\n"
    "v_mfma_f32_16x16x32_bf16 v[124:127], v[168:171], a[68:71], v[124:127]\n"
    "s_add_u32 s47, s51, s52\n"
    "s_add_u32 s51, s51, 512\n"
    "s_and_b32 s51, s51, 0xfff\n"
    "v_mov_b32 v25, s47\n"
    "v_mov_b32 v26, 4\n"
    "s_mov_b64 exec, 1\n"
    "global_atomic_add v25, v26, s[44:45]\n"
    "s_mov_b64 exec, -1\n"
    "v_mfma_f32_16x16x32_bf16 v[128:131], v[168:171], a[72:75], v[128:131]\n"
    "v_mfma_f32_16x16x32_bf16 v[132:135], v[168:171], a[76:79], v[132:135]\n"
    "global_load_dwordx4 v[168:171], v16, s[36:37] offset:1024\n"
    "v_mfma_f32_16x16x32_bf16 v[120:123], v[172:175], a[80:83], v[120:123]\n"
    "v_mfma_f32_16x16x32_bf16 v[124:127], v[172:175], a[84:87], v[124:127]\n"
    "v_mfma_f32_16x16x32_bf16 v[128:131], v[172:175], a[88:91], v[128:131]\n"
    "v_mfma_f32_16x16x32_bf16 v[132:135], v[172:175], a[92:95], v[132:135]\n"
    "global_load_dwordx4 v[172:175], v16, s[36:37] offset:2048\n"
    "v_add_u32 v16, 0x1800, v16\n"
    "s_waitcnt vmcnt(16)\n"
    "v_mfma_f32_16x16x32_bf16 v[120:123], v[176:179], a[96:99], v[120:123]\n"
    "v_mfma_f32_16x16x32_bf16 v[124:127], v[176:179], a[100:103], v[124:127]\n"
    "v_mfma_f32_16x16x32_bf16 v[128:131], v[176:179], a[104:107], v[128:131]\n"
    "v_mfma_f32_16x16x32_bf16 v[132:135], v[176:179], a[108:111], v[132:135]\n"
    "global_load_dwordx4 v[176:179], v16, s[36:37] offset:-3072\n"
    "v_mfma_f32_16x16x32_bf16 v[120:123], v[180:183], a[112:115], v[120:123]\n"
    "v_mfma_f32_16x16x32_bf16 v[124:127], v[180:183], a[116:119], v[124:127]\n"
    "v_mfma_f32_16x16x32_bf16 v[128:131], v[180:183], a[120:123], v[128:131]\n"
    "v_mfma_f32_16x16x32_bf16 v[132:135], v[180:183], a[124:127], v[132:135]\n"
    "global_load_dwordx4 v[180:183], v16, s[36:37] offset:-2048\n"
    "s_waitcnt vmcnt(16)\n"
    "v_mfma_f32_16x16x32_bf16 v[120:123], v[184:187], a[128:131], v[120:123]\n"
    "v_mfma_f32_16x16x32_bf16 v[124:127], v[184:187], a[132:135], v[124:127]\n"
    "v_mfma_f32_16x16x32_bf16 v[128:131], v[184:187], a[136:139], v[128:131]\n"
    "v_mfma_f32_16x16x32_bf16 v[132:135], v[184:187], a[140:143], v[132:135]\n"
    "global_load_dwordx4 v[184:187], v16, s[36:37] offset:-1024\n"
    "v_mfma_f32_16x16x32_bf16 v[120:123], v[188:191], a[144:147], v[120:123]\n"
    "v_mfma_f32_16x16x32_bf16 v[124:127], v[188:191], a[148:151], v[124:127]\n"
    "v_mfma_f32_16x16x32_bf16 v[128:131], v[188:191], a[152:155], v[128:131]\n"
    "v_mfma_f32_16x16x32_bf16 v[132:135], v[188:191], a[156:159], v[132:135]\n"
    "global_load_dwordx4 v[188:191], v16, s[36:37] offset:0\n"
    "s_waitcnt vmcnt(16)\n"
    "v_mfma_f32_16x16x32_bf16 v[120:123], v[192:195], a[160:163], v[120:123]\n"
    "v_mfma_f32_16x16x32_bf16 v[124:127], v[192:195], a[164:167], v[124:127]\n"
    "v_mfma_f32_16x16x32_bf16 v[128:131], v[192:195], a[168:171], v[128:131]\n"
    "v_mfma_f32_16x16x32_bf16 v[132:135], v[192:195], a[172:175], v[132:135]\n"
    "global_load_dwordx4 v[192:195], v16, s[36:37] offset:1024\n"
    "v_mfma_f32_16x16x32_bf16 v[120:123], v[196:199], a[176:179], v[120:123]\n"
    "v_mfma_f32_16x16x32_bf16 v[124:127], v[196:199], a[180:183], v[124:127]\n"
    "v_mfma_f32_16x16x32_bf16 v[128:131], v[196:199], a[184:187], v[128:131]\n"
    "v_mfma_f32_16x16x32_bf16 v[132:135], v[196:199], a[188:191], v[132:135]\n"
    "global_load_dwordx4 v[196:199], v16, s[36:37] offset:2048\n"
    "v_add_u32 v16, 0x1800, v16\n"
    "s_waitcnt vmcnt(16)\n"
    "v_mfma_f32_16x16x32_bf16 v[120:123], v[200:203], a[192:195], v[120:123]\n"
    "v_mfma_f32_16x16x32_bf16 v[124:127], v[200:203], a[196:199], v[124:127]\n"
    "v_mfma_f32_16x16x32_bf16 v[128:131], v[200:203], a[200:203], v[128:131]\n"
    "v_mfma_f32_16x16x32_bf16 v[132:135], v[200:203], a[204:207], v[132:135]\n"
    "global_load_dwordx4 v[200:203], v16, s[36:37] offset:-3072\n"
    "v_mfma_f32_16x16x32_bf16 v[120:123], v[204:207], a[208:211], v[120:123]\n"
    "v_mfma_f32_16x16x32_bf16 v[124:127], v[204:207], a[212:215], v[124:127]\n"
    "v_mfma_f32_16x16x32_bf16 v[128:131], v[204:207], a[216:219], v[128:131]\n"
    "v_mfma_f32_16x16x32_bf16 v[132:135], v[204:207], a[220:223], v[132:135]\n"
    "global_load_dwordx4 v[204:207], v16, s[36:37] offset:-2048\n"
    "s_waitcnt vmcnt(16)\n"
    "v_mfma_f32_16x16x32_bf16 v[120:123], v[208:211], a[224:227], v[120:123]\n"
    "v_readfirstlane_b32 s47, v22\n"
    "s_cmp_ge_u32 s47, s53\n"
    "s_cbranch_scc1 AOK0_%=\n"
    "s_mov_b32 s46, 0x100000\n"
    "AP0_%=:\n"
    "global_load_dwordx4 v[20:23], v24, s[64:65] sc1\n"
    "s_waitcnt vmcnt(0)\n"
    "v_readfirstlane_b32 s47, v22\n"
    "s_cmp_ge_u32 s47, s53\n"
    "s_cbranch_scc1 AP0_ok_%=\n"
    "s_sleep 1\n"
    "s_sub_u32 s46, s46, 1\n"
    "s_cmp_lg_u32 s46, 0\n"
    "s_cbranch_scc1 AP0_%=\n"
    "AP0_ok_%=:\n"
    "AOK0_%=:\n"
    "v_mfma_f32_16x16x32_bf16 v[124:127], v[208:211], a[228:231], v[124:127]\n"
    "s_cmp_lg_u32 s60, 0\n"
    "s_cbranch_scc1 XL0_%=\n"
    "s_add_u32 s62, s54, %[sxoff]\n"
    "s_addc_u32 s63, s55, 0\n"
    "global_load_dwordx4 v[104:107], v17, s[62:63] sc1\n"
    "s_add_u32 s62, s62, 9216\n"
    "s_addc_u32 s63, s63, 0\n"
    "global_load_dwordx4 v[108:111], v17, s[62:63] sc1\n"
    "s_add_u32 s62, s62, 9216\n"
    "s_addc_u32 s63, s63, 0\n"
    "global_load_dwordx4 v[112:115], v17, s[62:63] sc1\n"
    "s_add_u32 s62, s62, 9216\n"
    "s_addc_u32 s63, s63, 0\n"
    "global_load_dwordx4 v[116:119], v17, s[62:63] sc1\n"
    "XL0_%=:\n"
    "v_mfma_f32_16x16x32_bf16 v[128:131], v[208:211], a[232:235], v[128:131]\n"
    "global_load_dwordx4 v[20:23], v24, s[64:65] sc1\n"
    "v_mfma_f32_16x16x32_bf16 v[132:135], v[208:211], a[236:239], v[132:135]\n"
    "global_load_dwordx4 v[208:211], v16, s[36:37] offset:-1024\n"
    "v_mfma_f32_16x16x32_bf16 v[120:123], v[212:215], a[240:243], v[120:123]\n"
    "v_mfma_f32_16x16x32_bf16 v[124:127], v[212:215], a[244:247], v[124:127]\n"
    "v_mfma_f32_16x16x32_bf16 v[128:131], v[212:215], a[248:251], v[128:131]\n"
    "v_mfma_f32_16x16x32_bf16 v[132:135], v[212:215], a[252:255], v[132:135]\n"
    "global_load_dwordx4 v[212:215], v16, s[36:37] offset:0\n"
    "s_waitcnt vmcnt(16)\n"
    "v_mfma_f32_16x16x32_bf16 v[120:123], v[216:219], v[224:227], v[120:123]\n"
    "v_mfma_f32_16x16x32_bf16 v[124:127], v[216:219], v[228:231], v[124:127]\n"
    "v_mfma_f32_16x16x32_bf16 v[128:131], v[216:219], v[232:235], v[128:131]\n"
    "v_mfma_f32_16x16x32_bf16 v[132:135], v[216:219], v[236:239], v[132:135]\n"
    "global_load_dwordx4 v[216:219], v16, s[36:37] offset:1024\n"
    "v_mfma_f32_16x16x32_bf16 v[120:123], v[220:223], v[240:243], v[120:123]\n"
    "v_mfma_f32_16x16x32_bf16 v[124:127], v[220:223], v[244:247], v[124:127]\n"
    "v_mfma_f32_16x16x32_bf16 v[128:131], v[220:223], v[248:251], v[128:131]\n"
    "v_mfma_f32_16x16x32_bf16 v[132:135], v[220:223], v[252:255], v[132:135]\n"
    "global_load_dwordx4 v[220:223], v16, s[36:37] offset:2048\n"
    "v_add_u32 v16, 0x1800, v16\n"
    "s_waitcnt vmcnt(16)\n"
    "v_mfma_f32_16x16x32_bf16 v[136:139], v[152:155], a[0:3], 0\n"
    "v_mfma_f32_16x16x32_bf16 v[140:143], v[152:155], a[4:7], 0\n"
    "v_mfma_f32_16x16x32_bf16 v[144:147], v[152:155], a[8:11], 0\n"
    "v_mfma_f32_16x16x32_bf16 v[148:151], v[152:155], a[12:15], 0\n"
    "global_load_dwordx4 v[152:155], v16, s[36:37] offset:-3072\n"
    "v_mfma_f32_16x16x32_bf16 v[136:139], v[156:159], a[16:19], v[136:139]\n"
    "s_add_u32 s40, s48, s50\n"
    "s_addc_u32 s41, s49, 0\n"
    "v_mfma_f32_16x16x32_bf16 v[140:143], v[156:159], a[20:23], v[140:143]\n"
    "s_add_u32 s50, s50, 0x12000\n"
    "s_cmp_eq_u32 s50, 0x90000\n"
    "v_mfma_f32_16x16x32_bf16 v[144:147], v[156:159], a[24:27], v[144:147]\n"
    "s_cselect_b32 s50, 0, s50\n"
    "v_mfma_f32_16x16x32_bf16 v[148:151], v[156:159], a[28:31], v[148:151]\n"
    "global_load_dwordx4 v[156:159], v16, s[36:37] offset:-2048\n"
    "s_waitcnt vmcnt(16)\n"
    "v_mfma_f32_16x16x32_bf16 v[136:139], v[160:163], a[32:35], v[136:139]\n"
    "v_cvt_pk_bf16_f32 v56, v120, v121\n"
    "v_cvt_pk_bf16_f32 v57, v122, v123\n"
    "v_mfma_f32_16x16x32_bf16 v[140:143], v[160:163], a[36:39], v[140:143]\n"
    "v_cvt_pk_bf16_f32 v58, v124, v125\n"
    "v_cvt_pk_bf16_f32 v59, v126, v127\n"
    "v_mfma_f32_16x16x32_bf16 v[144:147], v[160:163], a[40:43], v[144:147]\n"
    "global_store_dwordx4 v17, v[56:59], s[40:41] offset:0 sc1\n"
    "v_mfma_f32_16x16x32_bf16 v[148:151], v[160:163], a[44:47], v[148:151]\n"
    "global_load_dwordx4 v[160:163], v16, s[36:37] offset:-1024\n"
    "v_mfma_f32_16x16x32_bf16 v[136:139], v[164:167], a[48:51], v[136:139]\n"
    "v_cvt_pk_bf16_f32 v60, v128, v129\n"
    "v_cvt_pk_bf16_f32 v61, v130, v131\n"
    "v_mfma_f32_16x16x32_bf16 v[140:143], v[164:167], a[52:55], v[140:143]\n"
    "v_cvt_pk_bf16_f32 v62, v132, v133\n"
    "v_cvt_pk_bf16_f32 v63, v134, v135\n"
    "v_mfma_f32_16x16x32_bf16 v[144:147], v[164:167], a[56:59], v[144:147]\n"
    "global_store_dwordx4 v17, v[60:63], s[40:41] offset:1024 sc1\n"
    "v_mfma_f32_16x16x32_bf16 v[148:151], v[164:167], a[60:63], v[148:151]\n"
    "global_load_dwordx4 v[164:167], v16, s[36:37] offset:0\n"
    "s_waitcnt vmcnt(16)\n"
    "v_mfma_f32_16x16x32_bf16 v[136:139], v[168:171], a[64:67], v[136:139]\n"
    "v_mfma_f32_16x16x32_bf16 v[140:143], v[168:171], a[68:71], v[140:143]\n"
    "s_add_u32 s47, s51, s52\n"
    "s_add_u32 s51, s51, 512\n"
    "s_and_b32 s51, s51, 0xfff\n"
    "v_mov_b32 v25, s47\n"
    "v_mov_b32 v26, 8\n"
    "s_mov_b64 exec, 1\n"
    "global_atomic_add v25, v26, s[44:45]\n"
    "s_mov_b64 exec, -1\n"
    "v_mfma_f32_16x16x32_bf16 v[144:147], v[168:171], a[72:75], v[144:147]\n"
    "v_mfma_f32_16x16x32_bf16 v[148:151], v[168:171], a[76:79], v[148:151]\n"
    "global_load_dwordx4 v[168:171], v16, s[36:37] offset:1024\n"
    "v_mfma_f32_16x16x32_bf16 v[136:139], v[172:175], a[80:83], v[136:139]\n"
    "v_mfma_f32_16x16x32_bf16 v[140:143], v[172:175], a[84:87], v[140:143]\n"
    "v_mfma_f32_16x16x32_bf16 v[144:147], v[172:175], a[88:91], v[144:147]\n"
    "v_mfma_f32_16x16x32_bf16 v[148:151], v[172:175], a[92:95], v[148:151]\n"
    "global_load_dwordx4 v[172:175], v16, s[36:37] offset:2048\n"
    "v_add_u32 v16, 0x1800, v16\n"
    "s_waitcnt vmcnt(16)\n"
    "v_mfma_f32_16x16x32_bf16 v[136:139], v[176:179], a[96:99], v[136:139]\n"
    "v_mfma_f32_16x16x32_bf16 v[140:143], v[176:179], a[100:103], v[140:143]\n"
    "v_mfma_f32_16x16x32_bf16 v[144:147], v[176:179], a[104:107], v[144:147]\n"
    "v_mfma_f32_16x16x32_bf16 v[148:151], v[176:179], a[108:111], v[148:151]\n"
    "global_load_dwordx4 v[176:179], v16, s[36:37] offset:-3072\n"
    "v_mfma_f32_16x16x32_bf16 v[136:139], v[180:183], a[112:115], v[136:139]\n"
    "v_mfma_f32_16x16x32_bf16 v[140:143], v[180:183], a[116:119], v[140:143]\n"
    "v_mfma_f32_16x16x32_bf16 v[144:147], v[180:183], a[120:123], v[144:147]\n"
    "v_mfma_f32_16x16x32_bf16 v[148:151], v[180:183], a[124:127], v[148:151]\n"
    "global_load_dwordx4 v[180:183], v16, s[36:37] offset:-2048\n"
    "s_waitcnt vmcnt(16)\n"
    "v_mfma_f32_16x16x32_bf16 v[136:139], v[184:187], a[128:131], v[136:139]\n"
    "v_mfma_f32_16x16x32_bf16 v[140:143], v[184:187], a[132:135], v[140:143]\n"
    "v_mfma_f32_16x16x32_bf16 v[144:147], v[184:187], a[136:139], v[144:147]\n"
    "v_mfma_f32_16x16x32_bf16 v[148:151], v[184:187], a[140:143], v[148:151]\n"
    "global_load_dwordx4 v[184:187], v16, s[36:37] offset:-1024\n"
    "v_mfma_f32_16x16x32_bf16 v[136:139], v[188:191], a[144:147], v[136:139]\n"
    "v_mfma_f32_16x16x32_bf16 v[140:143], v[188:191], a[148:151], v[140:143]\n"
    "v_mfma_f32_16x16x32_bf16 v[144:147], v[188:191], a[152:155], v[144:147]\n"
    "v_mfma_f32_16x16x32_bf16 v[148:151], v[188:191], a[156:159], v[148:151]\n"
    "global_load_dwordx4 v[188:191], v16, s[36:37] offset:0\n"
    "s_waitcnt vmcnt(16)\n"
    "v_mfma_f32_16x16x32_bf16 v[136:139], v[192:195], a[160:163], v[136:139]\n"
    "v_mfma_f32_16x16x32_bf16 v[140:143], v[192:195], a[164:167], v[140:143]\n"
    "v_mfma_f32_16x16x32_bf16 v[144:147], v[192:195], a[168:171], v[144:147]\n"
    "v_mfma_f32_16x16x32_bf16 v[148:151], v[192:195], a[172:175], v[148:151]\n"
    "global_load_dwordx4 v[192:195], v16, s[36:37] offset:1024\n"
    "v_mfma_f32_16x16x32_bf16 v[136:139], v[196:199], a[176:179], v[136:139]\n"
    "v_mfma_f32_16x16x32_bf16 v[140:143], v[196:199], a[180:183], v[140:143]\n"
    "v_mfma_f32_16x16x32_bf16 v[144:147], v[196:199], a[184:187], v[144:147]\n"
    "v_mfma_f32_16x16x32_bf16 v[148:151], v[196:199], a[188:191], v[148:151]\n"
    "global_load_dwordx4 v[196:199], v16, s[36:37] offset:2048\n"
    "v_add_u32 v16, 0x1800, v16\n"
    "s_waitcnt vmcnt(16)\n"
    "v_mfma_f32_16x16x32_bf16 v[136:139], v[200:203], a[192:195], v[136:139]\n"
    "v_mfma_f32_16x16x32_bf16 v[140:143], v[200:203], a[196:199], v[140:143]\n"
    "v_mfma_f32_16x16x32_bf16 v[144:147], v[200:203], a[200:203], v[144:147]\n"
    "v_mfma_f32_16x16x32_bf16 v[148:151], v[200:203], a[204:207], v[148:151]\n"
    "global_load_dwordx4 v[200:203], v16, s[36:37] offset:-3072\n"
    "v_mfma_f32_16x16x32_bf16 v[136:139], v[204:207], a[208:211], v[136:139]\n"
    "v_mfma_f32_16x16x32_bf16 v[140:143], v[204:207], a[212:215], v[140:143]\n"
    "v_mfma_f32_16x16x32_bf16 v[144:147], v[204:207], a[216:219], v[144:147]\n"
    "v_mfma_f32_16x16x32_bf16 v[148:151], v[204:207], a[220:223], v[148:151]\n"
    "global_load_dwordx4 v[204:207], v16, s[36:37] offset:-2048\n"
    "s_waitcnt vmcnt(16)\n"
    "v_mfma_f32_16x16x32_bf16 v[136:139], v[208:211], a[224:227], v[136:139]\n"
    "v_readfirstlane_b32 s47, v20\n"
    "s_and_b32 s47, s47, 2\n"
    "s_cmp_eq_u32 s47, 2\n"
    "s_cbranch_scc1 AOK1_%=\n"
    "s_mov_b32 s46, 0x100000\n"
    "AP1_%=:\n"
    "global_load_dwordx4 v[20:23], v24, s[64:65] sc1\n"
    "s_waitcnt vmcnt(0)\n"
    "v_readfirstlane_b32 s47, v20\n"
    "s_and_b32 s47, s47, 2\n"
    "s_cmp_eq_u32 s47, 2\n"
    "s_cbranch_scc1 AP1_ok_%=\n"
    "s_sleep 1\n"
    "s_sub_u32 s46, s46, 1\n"
    "s_cmp_lg_u32 s46, 0\n"
    "s_cbranch_scc1 AP1_%=\n"
    "AP1_ok_%=:\n"
    "AOK1_%=:\n"
    "v_mfma_f32_16x16x32_bf16 v[140:143], v[208:211], a[228:231], v[140:143]\n"
    "s_sub_u32 s61, %[ss], 1\n"
    "s_and_b32 s61, s61, 7\n"
    "s_mul_i32 s61, s61, 9216\n"
    "s_mov_b64 s[62:63], s[54:55]\n"
    "s_add_u32 s62, s62, s61\n"
    "s_addc_u32 s63, s63, 0\n"
    "global_load_dwordx4 v[88:91], v17, s[62:63] sc1\n"
    "global_load_dwordx4 v[92:95], v17, s[62:63] offset:1024 sc1\n"
    "v_mfma_f32_16x16x32_bf16 v[144:147], v[208:211], a[232:235], v[144:147]\n"
    "global_load_dwordx4 v[20:23], v24, s[64:65] sc1\n"
    "v_mfma_f32_16x16x32_bf16 v[148:151], v[208:211], a[236:239], v[148:151]\n"
    "global_load_dwordx4 v[208:211], v16, s[36:37] offset:-1024\n"
    "v_mfma_f32_16x16x32_bf16 v[136:139], v[212:215], a[240:243], v[136:139]\n"
    "v_mfma_f32_16x16x32_bf16 v[140:143], v[212:215], a[244:247], v[140:143]\n"
    "v_mfma_f32_16x16x32_bf16 v[144:147], v[212:215], a[248:251], v[144:147]\n"
    "v_mfma_f32_16x16x32_bf16 v[148:151], v[212:215], a[252:255], v[148:151]\n"
    "global_load_dwordx4 v[212:215], v16, s[36:37] offset:0\n"
    "s_waitcnt vmcnt(16)\n"
    "v_mfma_f32_16x16x32_bf16 v[136:139], v[216:219], v[224:227], v[136:139]\n"
    "v_mfma_f32_16x16x32_bf16 v[140:143], v[216:219], v[228:231], v[140:143]\n"
    "v_mfma_f32_16x16x32_bf16 v[144:147], v[216:219], v[232:235], v[144:147]\n"
    "s_cmp_lg_u32 s60, 0\n"
    "s_cbranch_scc1 XA0_%=\n"
    "v_mfma_f32_16x16x32_bf16 v[48:51], v[32:35], v[104:107], 0\n"
    "v_mfma_f32_16x16x32_bf16 v[52:55], v[36:39], v[104:107], 0\n"
    "v_mfma_f32_16x16x32_bf16 v[48:51], v[32:35], v[108:111], v[48:51]\n"
    "v_mfma_f32_16x16x32_bf16 v[52:55], v[36:39], v[108:111], v[52:55]\n"
    "v_mfma_f32_16x16x32_bf16 v[48:51], v[32:35], v[112:115], v[48:51]\n"
    "v_mfma_f32_16x16x32_bf16 v[52:55], v[36:39], v[112:115], v[52:55]\n"
    "v_mfma_f32_16x16x32_bf16 v[48:51], v[32:35], v[116:119], v[48:51]\n"
    "v_mfma_f32_16x16x32_bf16 v[52:55], v[36:39], v[116:119], v[52:55]\n"
    "XA0_%=:\n"
    "v_mfma_f32_16x16x32_bf16 v[148:151], v[216:219], v[236:239], v[148:151]\n"
    "global_load_dwordx4 v[216:219], v16, s[36:37] offset:1024\n"
    "v_mfma_f32_16x16x32_bf16 v[136:139], v[220:223], v[240:243], v[136:139]\n"
    "v_mfma_f32_16x16x32_bf16 v[140:143], v[220:223], v[244:247], v[140:143]\n"
    "v_mfma_f32_16x16x32_bf16 v[144:147], v[220:223], v[248:251], v[144:147]\n"
    "s_cmp_lg_u32 s60, 0\n"
    "s_cbranch_scc1 XL1_%=\n"
    "s_add_u32 s62, s54, %[sxoff]\n"
    "s_addc_u32 s63, s55, 0\n"
    "s_add_u32 s62, s62, 36864\n"
    "s_addc_u32 s63, s63, 0\n"
    "global_load_dwordx4 v[104:107], v17, s[62:63] sc1\n"
    "s_add_u32 s62, s62, 9216\n"
    "s_addc_u32 s63, s63, 0\n"
    "global_load_dwordx4 v[108:111], v17, s[62:63] sc1\n"
    "s_add_u32 s62, s62, 9216\n"
    "s_addc_u32 s63, s63, 0\n"
    "global_load_dwordx4 v[112:115], v17, s[62:63] sc1\n"
    "s_add_u32 s62, s62, 9216\n"
    "s_addc_u32 s63, s63, 0\n"
    "global_load_dwordx4 v[116:119], v17, s[62:63] sc1\n"
    "XL1_%=:\n"
    "v_mfma_f32_16x16x32_bf16 v[148:151], v[220:223], v[252:255], v[148:151]\n"
    "global_load_dwordx4 v[220:223], v16, s[36:37] offset:2048\n"
    "v_add_u32 v16, 0x1800, v16\n"
    "s_waitcnt vmcnt(16)\n"
    "v_mfma_f32_16x16x32_bf16 v[120:123], v[152:155], a[0:3], 0\n"
    "v_mfma_f32_16x16x32_bf16 v[124:127], v[152:155], a[4:7], 0\n"
    "v_mfma_f32_16x16x32_bf16 v[128:131], v[152:155], a[8:11], 0\n"
    "v_mfma_f32_16x16x32_bf16 v[132:135], v[152:155], a[12:15], 0\n"
    "global_load_dwordx4 v[152:155], v16, s[36:37] offset:-3072\n"
    "v_mfma_f32_16x16x32_bf16 v[120:123], v[156:159], a[16:19], v[120:123]\n"
    "s_add_u32 s40, s48, s50\n"
    "s_addc_u32 s41, s49, 0\n"
    "v_mfma_f32_16x16x32_bf16 v[124:127], v[156:159], a[20:23], v[124:127]\n"
    "s_add_u32 s50, s50, 0x12000\n"
    "s_cmp_eq_u32 s50, 0x90000\n"
    "v_mfma_f32_16x16x32_bf16 v[128:131], v[156:159], a[24:27], v[128:131]\n"
    "s_cselect_b32 s50, 0, s50\n"
    "v_mfma_f32_16x16x32_bf16 v[132:135], v[156:159], a[28:31], v[132:135]\n"
    "global_load_dwordx4 v[156:159], v16, s[36:37] offset:-2048\n"
    "s_waitcnt vmcnt(16)\n"
    "v_mfma_f32_16x16x32_bf16 v[120:123], v[160:163], a[32:35], v[120:123]\n"
    "v_cvt_pk_bf16_f32 v56, v136, v137\n"
    "v_cvt_pk_bf16_f32 v57, v138, v139\n"
    "v_mfma_f32_16x16x32_bf16 v[124:127], v[160:163], a[36:39], v[124:127]\n"
    "v_cvt_pk_bf16_f32 v58, v140, v141\n"
    "v_cvt_pk_bf16_f32 v59, v142, v143\n"
    "v_mfma_f32_16x16x32_bf16 v[128:131], v[160:163], a[40:43], v[128:131]\n"
    "global_store_dwordx4 v17, v[56:59], s[40:41] offset:0 sc1\n"
    "v_mfma_f32_16x16x32_bf16 v[132:135], v[160:163], a[44:47], v[132:135]\n"
    "global_load_dwordx4 v[160:163], v16, s[36:37] offset:-1024\n"
    "v_mfma_f32_16x16x32_bf16 v[120:123], v[164:167], a[48:51], v[120:123]\n"
    "v_cvt_pk_bf16_f32 v60, v144, v145\n"
    "v_cvt_pk_bf16_f32 v61, v146, v147\n"
    "v_mfma_f32_16x16x32_bf16 v[124:127], v[164:167], a[52:55], v[124:127]\n"
    "v_cvt_pk_bf16_f32 v62, v148, v149\n"
    "v_cvt_pk_bf16_f32 v63, v150, v151\n"
    "v_mfma_f32_16x16x32_bf16 v[128:131], v[164:167], a[56:59], v[128:131]\n"
    "global_store_dwordx4 v17, v[60:63], s[40:41] offset:1024 sc1\n"
    "v_mfma_f32_16x16x32_bf16 v[132:135], v[164:167], a[60:63], v[132:135]\n"
    "global_load_dwordx4 v[164:167], v16, s[36:37] offset:0\n"
    "s_waitcnt vmcnt(16)\n"
    "v_mfma_f32_16x16x32_bf16 v[120:123], v[168:171], a[64:67], v[120:123]\n"
    "v_mfma_f32_16x16x32_bf16 v[124:127], v[168:171], a[68:71], v[124:127]\n"
    "s_add_u32 s47, s51, s52\n"
    "s_add_u32 s51, s51, 512\n"
    "s_and_b32 s51, s51, 0xfff\n"
    "v_mov_b32 v25, s47\n"
    "v_mov_b32 v26, 16\n"
    "s_mov_b64 exec, 1\n"
    "global_atomic_add v25, v26, s[44:45]\n"
    "s_mov_b64 exec, -1\n"
    "v_mfma_f32_16x16x32_bf16 v[128:131], v[168:171], a[72:75], v[128:131]\n"
    "v_mfma_f32_16x16x32_bf16 v[132:135], v[168:171], a[76:79], v[132:135]\n"
    "global_load_dwordx4 v[168:171], v16, s[36:37] offset:1024\n"
    "v_mfma_f32_16x16x32_bf16 v[120:123], v[172:175], a[80:83], v[120:123]\n"
    "v_mfma_f32_16x16x32_bf16 v[124:127], v[172:175], a[84:87], v[124:127]\n"
    "v_mfma_f32_16x16x32_bf16 v[128:131], v[172:175], a[88:91], v[128:131]\n"
    "v_mfma_f32_16x16x32_bf16 v[132:135], v[172:175], a[92:95], v[132:135]\n"
    "global_load_dwordx4 v[172:175], v16, s[36:37] offset:2048\n"
    "v_add_u32 v16, 0x1800, v16\n"
    "s_waitcnt vmcnt(16)\n"
    "v_mfma_f32_16x16x32_bf16 v[120:123], v[176:179], a[96:99], v[120:123]\n"
    "v_mfma_f32_16x16x32_bf16 v[124:127], v[176:179], a[100:103], v[124:127]\n"
    "v_mfma_f32_16x16x32_bf16 v[128:131], v[176:179], a[104:107], v[128:131]\n"
    "v_mfma_f32_16x16x32_bf16 v[132:135], v[176:179], a[108:111], v[132:135]\n"
    "global_load_dwordx4 v[176:179], v16, s[36:37] offset:-3072\n"
    "v_mfma_f32_16x16x32_bf16 v[120:123], v[180:183], a[112:115], v[120:123]\n"
    "v_mfma_f32_16x16x32_bf16 v[124:127], v[180:183], a[116:119], v[124:127]\n"
    "v_mfma_f32_16x16x32_bf16 v[128:131], v[180:183], a[120:123], v[128:131]\n"
    "v_mfma_f32_16x16x32_bf16 v[132:135], v[180:183], a[124:127], v[132:135]\n"
    "global_load_dwordx4 v[180:183], v16, s[36:37] offset:-2048\n"
    "s_waitcnt vmcnt(16)\n"
    "v_mfma_f32_16x16x32_bf16 v[120:123], v[184:187], a[128:131], v[120:123]\n"
    "v_mfma_f32_16x16x32_bf16 v[124:127], v[184:187], a[132:135], v[124:127]\n"
    "v_mfma_f32_16x16x32_bf16 v[128:131], v[184:187], a[136:139], v[128:131]\n"
    "v_mfma_f32_16x16x32_bf16 v[132:135], v[184:187], a[140:143], v[132:135]\n"
    "global_load_dwordx4 v[184:187], v16, s[36:37] offset:-1024\n"
    "v_mfma_f32_16x16x32_bf16 v[120:123], v[188:191], a[144:147], v[120:123]\n"
    "v_mfma_f32_16x16x32_bf16 v[124:127], v[188:191], a[148:151], v[124:127]\n"
    "v_mfma_f32_16x16x32_bf16 v[128:131], v[188:191], a[152:155], v[128:131]\n"
    "v_mfma_f32_16x16x32_bf16 v[132:135], v[188:191], a[156:159], v[132:135]\n"
    "global_load_dwordx4 v[188:191], v16, s[36:37] offset:0\n"
    "s_waitcnt vmcnt(16)\n"
    "v_mfma_f32_16x16x32_bf16 v[120:123], v[192:195], a[160:163], v[120:123]\n"
    "v_xor_b32 v47, 32, v18\n"
    "ds_read_b64_tr_b16 v[28:29], v18\n"
    "ds_read_b64_tr_b16 v[30:31], v47\n"
    "v_mfma_f32_16x16x32_bf16 v[124:127], v[192:195], a[164:167], v[124:127]\n"
    "v_mfma_f32_16x16x32_bf16 v[128:131], v[192:195], a[168:171], v[128:131]\n"
    "v_mfma_f32_16x16x32_bf16 v[132:135], v[192:195], a[172:175], v[132:135]\n"
    "global_load_dwordx4 v[192:195], v16, s[36:37] offset:1024\n"
    "v_mfma_f32_16x16x32_bf16 v[120:123], v[196:199], a[176:179], v[120:123]\n"
    "v_mfma_f32_16x16x32_bf16 v[124:127], v[196:199], a[180:183], v[124:127]\n"
    "v_mfma_f32_16x16x32_bf16 v[128:131], v[196:199], a[184:187], v[128:131]\n"
    "v_mfma_f32_16x16x32_bf16 v[132:135], v[196:199], a[188:191], v[132:135]\n"
    "global_load_dwordx4 v[196:199], v16, s[36:37] offset:2048\n"
    "v_add_u32 v16, 0x1800, v16\n"
    "s_waitcnt vmcnt(16)\n"
    "v_mfma_f32_16x16x32_bf16 v[120:123], v[200:203], a[192:195], v[120:123]\n"
    "v_mfma_f32_16x16x32_bf16 v[124:127], v[200:203], a[196:199], v[124:127]\n"
    "v_mfma_f32_16x16x32_bf16 v[128:131], v[200:203], a[200:203], v[128:131]\n"
    "v_mfma_f32_16x16x32_bf16 v[132:135], v[200:203], a[204:207], v[132:135]\n"
    "global_load_dwordx4 v[200:203], v16, s[36:37] offset:-3072\n"
    "v_mfma_f32_16x16x32_bf16 v[120:123], v[204:207], a[208:211], v[120:123]\n"
    "v_mfma_f32_16x16x32_bf16 v[124:127], v[204:207], a[212:215], v[124:127]\n"
    "v_mfma_f32_16x16x32_bf16 v[128:131], v[204:207], a[216:219], v[128:131]\n"
    "v_mfma_f32_16x16x32_bf16 v[132:135], v[204:207], a[220:223], v[132:135]\n"
    "global_load_dwordx4 v[204:207], v16, s[36:37] offset:-2048\n"
    "s_waitcnt vmcnt(16)\n"
    "v_mfma_f32_16x16x32_bf16 v[120:123], v[208:211], a[224:227], v[120:123]\n"
    "v_readfirstlane_b32 s47, v20\n"
    "s_and_b32 s47, s47, 772\n"
    "s_cmp_eq_u32 s47, 772\n"
    "s_cbranch_scc1 AOK2_%=\n"
    "s_mov_b32 s46, 0x100000\n"
    "AP2_%=:\n"
    "global_load_dwordx4 v[20:23], v24, s[64:65] sc1\n"
    "s_waitcnt vmcnt(0)\n"
    "v_readfirstlane_b32 s47, v20\n"
    "s_and_b32 s47, s47, 772\n"
    "s_cmp_eq_u32 s47, 772\n"
    "s_cbranch_scc1 AP2_ok_%=\n"
    "s_sleep 1\n"
    "s_sub_u32 s46, s46, 1\n"
    "s_cmp_lg_u32 s46, 0\n"
    "s_cbranch_scc1 AP2_%=\n"
    "AP2_ok_%=:\n"
    "AOK2_%=:\n"
    "v_mfma_f32_16x16x32_bf16 v[124:127], v[208:211], a[228:231], v[124:127]\n"
    "s_sub_u32 s61, %[ss], 2\n"
    "s_and_b32 s61, s61, 7\n"
    "s_mul_i32 s61, s61, 9216\n"
    "s_mov_b64 s[62:63], s[54:55]\n"
    "s_add_u32 s62, s62, s61\n"
    "s_addc_u32 s63, s63, 0\n"
    "global_load_dwordx4 v[96:99], v17, s[62:63] sc1\n"
    "global_load_dwordx4 v[100:103], v17, s[62:63] offset:1024 sc1\n"
    "v_mfma_f32_16x16x32_bf16 v[128:131], v[208:211], a[232:235], v[128:131]\n"
    "global_load_dwordx4 v[20:23], v24, s[64:65] sc1\n"
    "v_mfma_f32_16x16x32_bf16 v[132:135], v[208:211], a[236:239], v[132:135]\n"
    "global_load_dwordx4 v[208:211], v16, s[36:37] offset:-1024\n"
    "v_mfma_f32_16x16x32_bf16 v[120:123], v[212:215], a[240:243], v[120:123]\n"
    "v_mfma_f32_16x16x32_bf16 v[124:127], v[212:215], a[244:247], v[124:127]\n"
    "v_mfma_f32_16x16x32_bf16 v[128:131], v[212:215], a[248:251], v[128:131]\n"
    "v_mfma_f32_16x16x32_bf16 v[132:135], v[212:215], a[252:255], v[132:135]\n"
    "v_mfma_f32_16x16x32_bf16 v[64:67], v[32:35], v[88:91], 0\n"
    "v_mfma_f32_16x16x32_bf16 v[68:71], v[36:39], v[88:91], 0\n"
    "v_mfma_f32_16x16x32_bf16 v[72:75], v[32:35], v[92:95], 0\n"
    "v_mfma_f32_16x16x32_bf16 v[76:79], v[36:39], v[92:95], 0\n"
    "global_load_dwordx4 v[212:215], v16, s[36:37] offset:0\n"
    "s_waitcnt vmcnt(16)\n"
    "v_mfma_f32_16x16x32_bf16 v[120:123], v[216:219], v[224:227], v[120:123]\n"
    "v_mfma_f32_16x16x32_bf16 v[124:127], v[216:219], v[228:231], v[124:127]\n"
    "v_mfma_f32_16x16x32_bf16 v[128:131], v[216:219], v[232:235], v[128:131]\n"
    "s_cmp_lg_u32 s60, 0\n"
    "s_cbranch_scc1 XA1_%=\n"
    "v_mfma_f32_16x16x32_bf16 v[48:51], v[32:35], v[104:107], v[48:51]\n"
    "v_mfma_f32_16x16x32_bf16 v[52:55], v[36:39], v[104:107], v[52:55]\n"
    "v_mfma_f32_16x16x32_bf16 v[48:51], v[32:35], v[108:111], v[48:51]\n"
    "v_mfma_f32_16x16x32_bf16 v[52:55], v[36:39], v[108:111], v[52:55]\n"
    "v_mfma_f32_16x16x32_bf16 v[48:51], v[32:35], v[112:115], v[48:51]\n"
    "v_mfma_f32_16x16x32_bf16 v[52:55], v[36:39], v[112:115], v[52:55]\n"
    "v_mfma_f32_16x16x32_bf16 v[48:51], v[32:35], v[116:119], v[48:51]\n"
    "v_mfma_f32_16x16x32_bf16 v[52:55], v[36:39], v[116:119], v[52:55]\n"
    "XA1_%=:\n"
    "v_mfma_f32_16x16x32_bf16 v[132:135], v[216:219], v[236:239], v[132:135]\n"
    "global_load_dwordx4 v[216:219], v16, s[36:37] offset:1024\n"
    "v_mfma_f32_16x16x32_bf16 v[120:123], v[220:223], v[240:243], v[120:123]\n"
    "v_mfma_f32_16x16x32_bf16 v[124:127], v[220:223], v[244:247], v[124:127]\n"
    "v_mfma_f32_16x16x32_bf16 v[128:131], v[220:223], v[248:251], v[128:131]\n"
    "v_mfma_f32_16x16x32_bf16 v[132:135], v[220:223], v[252:255], v[132:135]\n"
    "global_load_dwordx4 v[220:223], v16, s[36:37] offset:2048\n"
    "v_add_u32 v16, 0x1800, v16\n"
    "s_waitcnt vmcnt(16)\n"
    "v_mfma_f32_16x16x32_bf16 v[136:139], v[152:155], a[0:3], 0\n"
    "v_mfma_f32_16x16x32_bf16 v[140:143], v[152:155], a[4:7], 0\n"
    "v_mfma_f32_16x16x32_bf16 v[144:147], v[152:155], a[8:11], 0\n"
    "v_mfma_f32_16x16x32_bf16 v[148:151], v[152:155], a[12:15], 0\n"
    "global_load_dwordx4 v[152:155], v16, s[36:37] offset:-3072\n"
    "v_mfma_f32_16x16x32_bf16 v[136:139], v[156:159], a[16:19], v[136:139]\n"
    "s_add_u32 s40, s48, s50\n"
    "s_addc_u32 s41, s49, 0\n"
    "v_mfma_f32_16x16x32_bf16 v[140:143], v[156:159], a[20:23], v[140:143]\n"
    "s_add_u32 s50, s50, 0x12000\n"
    "s_cmp_eq_u32 s50, 0x90000\n"
    "v_mfma_f32_16x16x32_bf16 v[144:147], v[156:159], a[24:27], v[144:147]\n"
    "s_cselect_b32 s50, 0, s50\n"
    "v_cvt_pk_bf16_f32 v56, v120, v121\n"
    "v_mfma_f32_16x16x32_bf16 v[148:151], v[156:159], a[28:31], v[148:151]\n"
    "v_cvt_pk_bf16_f32 v57, v122, v123\n"
    "global_load_dwordx4 v[156:159], v16, s[36:37] offset:-2048\n"
    "s_waitcnt vmcnt(16)\n"
    "v_mfma_f32_16x16x32_bf16 v[136:139], v[160:163], a[32:35], v[136:139]\n"
    "v_cvt_pk_bf16_f32 v58, v124, v125\n"
    "v_cvt_pk_bf16_f32 v59, v126, v127\n"
    "v_mfma_f32_16x16x32_bf16 v[140:143], v[160:163], a[36:39], v[140:143]\n"
    "v_min_u32 v56, -2, v56\n"
    "v_min_u32 v57, -2, v57\n"
    "v_mfma_f32_16x16x32_bf16 v[144:147], v[160:163], a[40:43], v[144:147]\n"
    "v_min_u32 v58, -2, v58\n"
    "v_min_u32 v59, -2, v59\n"
    "v_mfma_f32_16x16x32_bf16 v[148:151], v[160:163], a[44:47], v[148:151]\n"
    "global_store_dwordx4 v17, v[56:59], s[40:41] offset:0 sc1\n"
    "global_load_dwordx4 v[160:163], v16, s[36:37] offset:-1024\n"
    "v_mfma_f32_16x16x32_bf16 v[136:139], v[164:167], a[48:51], v[136:139]\n"
    "v_cvt_pk_bf16_f32 v60, v128, v129\n"
    "v_cvt_pk_bf16_f32 v61, v130, v131\n"
    "v_mfma_f32_16x16x32_bf16 v[140:143], v[164:167], a[52:55], v[140:143]\n"
    "v_cvt_pk_bf16_f32 v62, v132, v133\n"
    "v_cvt_pk_bf16_f32 v63, v134, v135\n"
    "v_mfma_f32_16x16x32_bf16 v[144:147], v[164:167], a[56:59], v[144:147]\n"
    "v_min_u32 v60, -2, v60\n"
    "v_min_u32 v61, -2, v61\n"
    "v_mfma_f32_16x16x32_bf16 v[148:151], v[164:167], a[60:63], v[148:151]\n"
    "v_min_u32 v62, -2, v62\n"
    "global_load_dwordx4 v[164:167], v16, s[36:37] offset:0\n"
    "s_waitcnt vmcnt(16)\n"
    "v_mfma_f32_16x16x32_bf16 v[136:139], v[168:171], a[64:67], v[136:139]\n"
    "v_min_u32 v63, -2, v63\n"
    "global_store_dwordx4 v17, v[60:63], s[40:41] offset:1024 sc1\n"
    "v_mfma_f32_16x16x32_bf16 v[140:143], v[168:171], a[68:71], v[140:143]\n"
    "s_add_u32 s47, s51, s52\n"
    "s_add_u32 s51, s51, 512\n"
    "s_and_b32 s51, s51, 0xfff\n"
    "v_mov_b32 v25, s47\n"
    "v_mov_b32 v26, 32\n"
    "s_mov_b64 exec, 1\n"
    "global_atomic_add v25, v26, s[44:45]\n"
    "s_mov_b64 exec, -1\n"
    "v_mfma_f32_16x16x32_bf16 v[144:147], v[168:171], a[72:75], v[144:147]\n"
    "v_mfma_f32_16x16x32_bf16 v[148:151], v[168:171], a[76:79], v[148:151]\n"
    "global_load_dwordx4 v[168:171], v16, s[36:37] offset:1024\n"
    "v_mfma_f32_16x16x32_bf16 v[136:139], v[172:175], a[80:83], v[136:139]\n"
    "v_mfma_f32_16x16x32_bf16 v[140:143], v[172:175], a[84:87], v[140:143]\n"
    "v_mfma_f32_16x16x32_bf16 v[144:147], v[172:175], a[88:91], v[144:147]\n"
    "v_mfma_f32_16x16x32_bf16 v[148:151], v[172:175], a[92:95], v[148:151]\n"
    "global_load_dwordx4 v[172:175], v16, s[36:37] offset:2048\n"
    "v_add_u32 v16, 0x1800, v16\n"
    "s_waitcnt vmcnt(16)\n"
    "v_mfma_f32_16x16x32_bf16 v[136:139], v[176:179], a[96:99], v[136:139]\n"
    "s_waitcnt lgkmcnt(0)\n"
    "v_mfma_f32_16x16x32_bf16 v[140:143], v[176:179], a[100:103], v[140:143]\n"
    "s_cmp_lg_u32 s60, 0\n"
    "s_cbranch_scc1 XF_%=\n"
    "v_lshlrev_b32 v112, 16, v42\n"
    "v_and_b32 v113, 0xffff0000, v42\n"
    "v_lshlrev_b32 v114, 16, v43\n"
    "v_and_b32 v115, 0xffff0000, v43\n"
    "v_cvt_pk_bf16_f32 v116, v48, v49\n"
    "v_lshlrev_b32 v117, 16, v116\n"
    "v_and_b32 v118, 0xffff0000, v116\n"
    "v_add_f32 v117, v117, v112\n"
    "v_add_f32 v118, v118, v113\n"
    "v_cvt_pk_bf16_f32 v116, v117, v118\n"
    "v_lshlrev_b32 v117, 16, v116\n"
    "v_and_b32 v118, 0xffff0000, v116\n"
    "v_lshlrev_b32 v119, 16, v28\n"
    "v_and_b32 v116, 0xffff0000, v28\n"
    "v_add_f32 v117, v117, v119\n"
    "v_add_f32 v118, v118, v116\n"
    "v_cvt_pk_bf16_f32 v104, v117, v118\n"
    "v_cvt_pk_bf16_f32 v116, v50, v51\n"
    "v_lshlrev_b32 v117, 16, v116\n"
    "v_and_b32 v118, 0xffff0000, v116\n"
    "v_add_f32 v117, v117, v114\n"
    "v_add_f32 v118, v118, v115\n"
    "v_cvt_pk_bf16_f32 v116, v117, v118\n"
    "v_lshlrev_b32 v117, 16, v116\n"
    "v_and_b32 v118, 0xffff0000, v116\n"
    "v_lshlrev_b32 v119, 16, v29\n"
    "v_and_b32 v116, 0xffff0000, v29\n"
    "v_add_f32 v117, v117, v119\n"
    "v_add_f32 v118, v118, v116\n"
    "v_cvt_pk_bf16_f32 v105, v117, v118\n"
    "v_cvt_pk_bf16_f32 v116, v52, v53\n"
    "v_lshlrev_b32 v117, 16, v116\n"
    "v_and_b32 v118, 0xffff0000, v116\n"
    "v_add_f32 v117, v117, v112\n"
    "v_add_f32 v118, v118, v113\n"
    "v_cvt_pk_bf16_f32 v116, v117, v118\n"
    "v_lshlrev_b32 v117, 16, v116\n"
    "v_and_b32 v118, 0xffff0000, v116\n"
    "v_lshlrev_b32 v119, 16, v30\n"
    "v_and_b32 v116, 0xffff0000, v30\n"
    "v_add_f32 v117, v117, v119\n"
    "v_add_f32 v118, v118, v116\n"
    "v_cvt_pk_bf16_f32 v106, v117, v118\n"
    "v_cvt_pk_bf16_f32 v116, v54, v55\n"
    "v_lshlrev_b32 v117, 16, v116\n"
    "v_and_b32 v118, 0xffff0000, v116\n"
    "v_add_f32 v117, v117, v114\n"
    "v_add_f32 v118, v118, v115\n"
    "v_cvt_pk_bf16_f32 v116, v117, v118\n"
    "v_lshlrev_b32 v117, 16, v116\n"
    "v_and_b32 v118, 0xffff0000, v116\n"
    "v_lshlrev_b32 v119, 16, v31\n"
    "v_and_b32 v116, 0xffff0000, v31\n"
    "v_add_f32 v117, v117, v119\n"
    "v_add_f32 v118, v118, v116\n"
    "v_cvt_pk_bf16_f32 v107, v117, v118\n"
    "v_add_u32 v44, 0x4800, v19\n"
    "global_store_dwordx2 v19, v[104:105], s[56:57]\n"
    "global_store_dwordx2 v44, v[106:107], s[56:57]\n"
    "XF_%=:\n"
    "v_mfma_f32_16x16x32_bf16 v[144:147], v[176:179], a[104:107], v[144:147]\n"
    "v_mfma_f32_16x16x32_bf16 v[148:151], v[176:179], a[108:111], v[148:151]\n"
    "global_load_dwordx4 v[176:179], v16, s[36:37] offset:-3072\n"
    "v_mfma_f32_16x16x32_bf16 v[136:139], v[180:183], a[112:115], v[136:139]\n"
    "v_mfma_f32_16x16x32_bf16 v[140:143], v[180:183], a[116:119], v[140:143]\n"
    "v_mfma_f32_16x16x32_bf16 v[144:147], v[180:183], a[120:123], v[144:147]\n"
    "v_mfma_f32_16x16x32_bf16 v[148:151], v[180:183], a[124:127], v[148:151]\n"
    "global_load_dwordx4 v[180:183], v16, s[36:37] offset:-2048\n"
    "s_waitcnt vmcnt(16)\n"
    "v_mfma_f32_16x16x32_bf16 v[136:139], v[184:187], a[128:131], v[136:139]\n"
    "v_mfma_f32_16x16x32_bf16 v[140:143], v[184:187], a[132:135], v[140:143]\n"
    "v_mfma_f32_16x16x32_bf16 v[144:147], v[184:187], a[136:139], v[144:147]\n"
    "v_mfma_f32_16x16x32_bf16 v[148:151], v[184:187], a[140:143], v[148:151]\n"
    "global_load_dwordx4 v[184:187], v16, s[36:37] offset:-1024\n"
    "v_mfma_f32_16x16x32_bf16 v[136:139], v[188:191], a[144:147], v[136:139]\n"
    "v_mfma_f32_16x16x32_bf16 v[140:143], v[188:191], a[148:151], v[140:143]\n"
    "v_mfma_f32_16x16x32_bf16 v[144:147], v[188:191], a[152:155], v[144:147]\n"
    "v_mfma_f32_16x16x32_bf16 v[148:151], v[188:191], a[156:159], v[148:151]\n"
    "global_load_dwordx4 v[188:191], v16, s[36:37] offset:0\n"
    "s_waitcnt vmcnt(16)\n"
    "v_mfma_f32_16x16x32_bf16 v[136:139], v[192:195], a[160:163], v[136:139]\n"
    "v_mfma_f32_16x16x32_bf16 v[140:143], v[192:195], a[164:167], v[140:143]\n"
    "v_mfma_f32_16x16x32_bf16 v[144:147], v[192:195], a[168:171], v[144:147]\n"
    "v_mfma_f32_16x16x32_bf16 v[148:151], v[192:195], a[172:175], v[148:151]\n"
    "global_load_dwordx4 v[192:195], v16, s[36:37] offset:1024\n"
    "v_mfma_f32_16x16x32_bf16 v[136:139], v[196:199], a[176:179], v[136:139]\n"
    "v_mfma_f32_16x16x32_bf16 v[140:143], v[196:199], a[180:183], v[140:143]\n"
    "v_mfma_f32_16x16x32_bf16 v[144:147], v[196:199], a[184:187], v[144:147]\n"
    "v_mfma_f32_16x16x32_bf16 v[148:151], v[196:199], a[188:191], v[148:151]\n"
    "global_load_dwordx4 v[196:199], v16, s[36:37] offset:2048\n"
    "v_add_u32 v16, 0x1800, v16\n"
    "s_waitcnt vmcnt(16)\n"
    "v_mfma_f32_16x16x32_bf16 v[136:139], v[200:203], a[192:195], v[136:139]\n"
    "v_mfma_f32_16x16x32_bf16 v[140:143], v[200:203], a[196:199], v[140:143]\n"
    "v_mfma_f32_16x16x32_bf16 v[144:147], v[200:203], a[200:203], v[144:147]\n"
    "v_mfma_f32_16x16x32_bf16 v[148:151], v[200:203], a[204:207], v[148:151]\n"
    "global_load_dwordx4 v[200:203], v16, s[36:37] offset:-3072\n"
    "v_mfma_f32_16x16x32_bf16 v[136:139], v[204:207], a[208:211], v[136:139]\n"
    "v_mfma_f32_16x16x32_bf16 v[140:143], v[204:207], a[212:215], v[140:143]\n"
    "v_mfma_f32_16x16x32_bf16 v[144:147], v[204:207], a[216:219], v[144:147]\n"
    "v_mfma_f32_16x16x32_bf16 v[148:151], v[204:207], a[220:223], v[148:151]\n"
    "global_load_dwordx4 v[204:207], v16, s[36:37] offset:-2048\n"
    "s_waitcnt vmcnt(16)\n"
    "v_mfma_f32_16x16x32_bf16 v[136:139], v[208:211], a[224:227], v[136:139]\n"
    "v_readfirstlane_b32 s47, v20\n"
    "s_and_b32 s47, s47, 8\n"
    "s_cmp_eq_u32 s47, 8\n"
    "s_cbranch_scc1 AOK3_%=\n"
    "s_mov_b32 s46, 0x100000\n"
    "AP3_%=:\n"
    "global_load_dwordx4 v[20:23], v24, s[64:65] sc1\n"
    "s_waitcnt vmcnt(0)\n"
    "v_readfirstlane_b32 s47, v20\n"
    "s_and_b32 s47, s47, 8\n"
    "s_cmp_eq_u32 s47, 8\n"
    "s_cbranch_scc1 AP3_ok_%=\n"
    "s_sleep 1\n"
    "s_sub_u32 s46, s46, 1\n"
    "s_cmp_lg_u32 s46, 0\n"
    "s_cbranch_scc1 AP3_%=\n"
    "AP3_ok_%=:\n"
    "AOK3_%=:\n"
    "v_mfma_f32_16x16x32_bf16 v[140:143], v[208:211], a[228:231], v[140:143]\n"
    "s_sub_u32 s61, %[ss], 3\n"
    "s_and_b32 s61, s61, 7\n"
    "s_mul_i32 s61, s61, 9216\n"
    "s_mov_b64 s[62:63], s[54:55]\n"
    "s_add_u32 s62, s62, s61\n"
    "s_addc_u32 s63, s63, 0\n"
    "global_load_dwordx4 v[80:83], v17, s[62:63] sc1\n"
    "global_load_dwordx4 v[84:87], v17, s[62:63] offset:1024 sc1\n"
    "v_mfma_f32_16x16x32_bf16 v[144:147], v[208:211], a[232:235], v[144:147]\n"
    "global_load_dwordx4 v[20:23], v24, s[64:65] sc1\n"
    "v_mfma_f32_16x16x32_bf16 v[148:151], v[208:211], a[236:239], v[148:151]\n"
    "global_load_dwordx4 v[208:211], v16, s[36:37] offset:-1024\n"
    "v_mfma_f32_16x16x32_bf16 v[136:139], v[212:215], a[240:243], v[136:139]\n"
    "v_mfma_f32_16x16x32_bf16 v[140:143], v[212:215], a[244:247], v[140:143]\n"
    "v_mfma_f32_16x16x32_bf16 v[144:147], v[212:215], a[248:251], v[144:147]\n"
    "v_mfma_f32_16x16x32_bf16 v[148:151], v[212:215], a[252:255], v[148:151]\n"
    "v_mfma_f32_16x16x32_bf16 v[64:67], v[32:35], v[96:99], v[64:67]\n"
    "v_mfma_f32_16x16x32_bf16 v[68:71], v[36:39], v[96:99], v[68:71]\n"
    "v_mfma_f32_16x16x32_bf16 v[72:75], v[32:35], v[100:103], v[72:75]\n"
    "v_mfma_f32_16x16x32_bf16 v[76:79], v[36:39], v[100:103], v[76:79]\n"
    "global_load_dwordx4 v[212:215], v16, s[36:37] offset:0\n"
    "s_waitcnt vmcnt(16)\n"
    "v_mfma_f32_16x16x32_bf16 v[136:139], v[216:219], v[224:227], v[136:139]\n"
    "v_mfma_f32_16x16x32_bf16 v[140:143], v[216:219], v[228:231], v[140:143]\n"
    "v_mfma_f32_16x16x32_bf16 v[144:147], v[216:219], v[232:235], v[144:147]\n"
    "v_mfma_f32_16x16x32_bf16 v[148:151], v[216:219], v[236:239], v[148:151]\n"
    "global_load_dwordx4 v[216:219], v16, s[36:37] offset:1024\n"
    "v_mfma_f32_16x16x32_bf16 v[136:139], v[220:223], v[240:243], v[136:139]\n"
    "v_mfma_f32_16x16x32_bf16 v[140:143], v[220:223], v[244:247], v[140:143]\n"
    "v_mfma_f32_16x16x32_bf16 v[144:147], v[220:223], v[248:251], v[144:147]\n"
    "v_mfma_f32_16x16x32_bf16 v[148:151], v[220:223], v[252:255], v[148:151]\n"
    "global_load_dwordx4 v[220:223], v16, s[36:37] offset:2048\n"
    "v_add_u32 v16, 0x1800, v16\n"
    "v_mfma_f32_16x16x32_bf16 v[120:123], v[152:155], a[0:3], v[64:67]\n"
    "v_mfma_f32_16x16x32_bf16 v[124:127], v[152:155], a[4:7], v[68:71]\n"
    "v_mfma_f32_16x16x32_bf16 v[128:131], v[152:155], a[8:11], v[72:75]\n"
    "v_mfma_f32_16x16x32_bf16 v[132:135], v[152:155], a[12:15], v[76:79]\n"
    "v_mfma_f32_16x16x32_bf16 v[120:123], v[156:159], a[16:19], v[120:123]\n"
    "s_add_u32 s40, s48, s50\n"
    "s_addc_u32 s41, s49, 0\n"
    "v_mfma_f32_16x16x32_bf16 v[124:127], v[156:159], a[20:23], v[124:127]\n"
    "s_add_u32 s50, s50, 0x12000\n"
    "s_cmp_eq_u32 s50, 0x90000\n"
    "v_mfma_f32_16x16x32_bf16 v[128:131], v[156:159], a[24:27], v[128:131]\n"
    "s_cselect_b32 s50, 0, s50\n"
    "v_cvt_pk_bf16_f32 v56, v136, v137\n"
    "v_mfma_f32_16x16x32_bf16 v[132:135], v[156:159], a[28:31], v[132:135]\n"
    "v_cvt_pk_bf16_f32 v57, v138, v139\n"
    "v_mfma_f32_16x16x32_bf16 v[120:123], v[160:163], a[32:35], v[120:123]\n"
    "v_cvt_pk_bf16_f32 v58, v140, v141\n"
    "v_cvt_pk_bf16_f32 v59, v142, v143\n"
    "v_mfma_f32_16x16x32_bf16 v[124:127], v[160:163], a[36:39], v[124:127]\n"
    "v_min_u32 v56, -2, v56\n"
    "v_min_u32 v57, -2, v57\n"
    "v_mfma_f32_16x16x32_bf16 v[128:131], v[160:163], a[40:43], v[128:131]\n"
    "v_min_u32 v58, -2, v58\n"
    "v_min_u32 v59, -2, v59\n"
    "v_mfma_f32_16x16x32_bf16 v[132:135], v[160:163], a[44:47], v[132:135]\n"
    "global_store_dwordx4 v17, v[56:59], s[40:41] offset:0 sc1\n"
    "v_mfma_f32_16x16x32_bf16 v[120:123], v[164:167], a[48:51], v[120:123]\n"
    "v_cvt_pk_bf16_f32 v60, v144, v145\n"
    "v_cvt_pk_bf16_f32 v61, v146, v147\n"
    "v_mfma_f32_16x16x32_bf16 v[124:127], v[164:167], a[52:55], v[124:127]\n"
    "v_cvt_pk_bf16_f32 v62, v148, v149\n"
    "v_cvt_pk_bf16_f32 v63, v150, v151\n"
    "v_mfma_f32_16x16x32_bf16 v[128:131], v[164:167], a[56:59], v[128:131]\n"
    "v_min_u32 v60, -2, v60\n"
    "v_min_u32 v61, -2, v61\n"
    "v_mfma_f32_16x16x32_bf16 v[132:135], v[164:167], a[60:63], v[132:135]\n"
    "v_min_u32 v62, -2, v62\n"
    "s_waitcnt vmcnt(16)\n"
    "v_mfma_f32_16x16x32_bf16 v[120:123], v[168:171], a[64:67], v[120:123]\n"
    "v_min_u32 v63, -2, v63\n"
    "global_store_dwordx4 v17, v[60:63], s[40:41] offset:1024 sc1\n"
    "v_mfma_f32_16x16x32_bf16 v[124:127], v[168:171], a[68:71], v[124:127]\n"
    "v_xor_b32 v44, 32, %[vho]\n"
    "v_xor_b32 v45, 64, %[vho]\n"
    "v_xor_b32 v46, 96, %[vho]\n"
    "ds_read_b64_tr_b16 v[48:49], %[vho]\n"
    "ds_read_b64_tr_b16 v[50:51], v44\n"
    "ds_read_b64_tr_b16 v[52:53], v45\n"
    "ds_read_b64_tr_b16 v[54:55], v46\n"
    "v_mfma_f32_16x16x32_bf16 v[128:131], v[168:171], a[72:75], v[128:131]\n"
    "v_mfma_f32_16x16x32_bf16 v[132:135], v[168:171], a[76:79], v[132:135]\n"
    "v_mfma_f32_16x16x32_bf16 v[120:123], v[172:175], a[80:83], v[120:123]\n"
    "s_waitcnt vmcnt(6)\n"
    "v_readfirstlane_b32 s47, v20\n"
    "s_and_b32 s47, s47, 48\n"
    "s_cmp_eq_u32 s47, 48\n"
    "s_cbranch_scc1 AOK4_%=\n"
    "s_mov_b32 s46, 0x100000\n"
    "AP4_%=:\n"
    "global_load_dwordx4 v[20:23], v24, s[64:65] sc1\n"
    "s_waitcnt vmcnt(0)\n"
    "v_readfirstlane_b32 s47, v20\n"
    "s_and_b32 s47, s47, 48\n"
    "s_cmp_eq_u32 s47, 48\n"
    "s_cbranch_scc1 AP4_ok_%=\n"
    "s_sleep 1\n"
    "s_sub_u32 s46, s46, 1\n"
    "s_cmp_lg_u32 s46, 0\n"
    "s_cbranch_scc1 AP4_%=\n"
    "AP4_ok_%=:\n"
    "AOK4_%=:\n"
    "v_mfma_f32_16x16x32_bf16 v[124:127], v[172:175], a[84:87], v[124:127]\n"
    "s_sub_u32 s61, %[ss], 4\n"
    "s_and_b32 s61, s61, 7\n"
    "s_mul_i32 s61, s61, 9216\n"
    "s_mov_b64 s[62:63], s[54:55]\n"
    "s_add_u32 s62, s62, s61\n"
    "s_addc_u32 s63, s63, 0\n"
    "global_load_dwordx4 v[88:91], v17, s[62:63] sc1\n"
    "global_load_dwordx4 v[92:95], v17, s[62:63] offset:1024 sc1\n"
    "v_mfma_f32_16x16x32_bf16 v[128:131], v[172:175], a[88:91], v[128:131]\n"
    "s_sub_u32 s61, %[ss], 5\n"
    "s_and_b32 s61, s61, 7\n"
    "s_mul_i32 s61, s61, 9216\n"
    "s_mov_b64 s[62:63], s[54:55]\n"
    "s_add_u32 s62, s62, s61\n"
    "s_addc_u32 s63, s63, 0\n"
    "global_load_dwordx4 v[96:99], v17, s[62:63] sc1\n"
    "global_load_dwordx4 v[100:103], v17, s[62:63] offset:1024 sc1\n"
    "v_mfma_f32_16x16x32_bf16 v[132:135], v[172:175], a[92:95], v[132:135]\n"
    "v_mfma_f32_16x16x32_bf16 v[120:123], v[176:179], a[96:99], v[120:123]\n"
    "v_mfma_f32_16x16x32_bf16 v[124:127], v[176:179], a[100:103], v[124:127]\n"
    "v_mfma_f32_16x16x32_bf16 v[128:131], v[176:179], a[104:107], v[128:131]\n"
    "v_mfma_f32_16x16x32_bf16 v[120:123], v[32:35], v[80:83], v[120:123]\n"
    "v_mfma_f32_16x16x32_bf16 v[124:127], v[36:39], v[80:83], v[124:127]\n"
    "v_mfma_f32_16x16x32_bf16 v[128:131], v[32:35], v[84:87], v[128:131]\n"
    "v_mfma_f32_16x16x32_bf16 v[132:135], v[36:39], v[84:87], v[132:135]\n"
    "v_mfma_f32_16x16x32_bf16 v[132:135], v[176:179], a[108:111], v[132:135]\n"
    "v_mfma_f32_16x16x32_bf16 v[120:123], v[180:183], a[112:115], v[120:123]\n"
    "v_mfma_f32_16x16x32_bf16 v[124:127], v[180:183], a[116:119], v[124:127]\n"
    "v_mfma_f32_16x16x32_bf16 v[128:131], v[180:183], a[120:123], v[128:131]\n"
    "v_mfma_f32_16x16x32_bf16 v[132:135], v[180:183], a[124:127], v[132:135]\n"
    "v_mfma_f32_16x16x32_bf16 v[120:123], v[184:187], a[128:131], v[120:123]\n"
    "v_mfma_f32_16x16x32_bf16 v[124:127], v[184:187], a[132:135], v[124:127]\n"
    "v_mfma_f32_16x16x32_bf16 v[128:131], v[184:187], a[136:139], v[128:131]\n"
    "v_mfma_f32_16x16x32_bf16 v[132:135], v[184:187], a[140:143], v[132:135]\n"
    "v_mfma_f32_16x16x32_bf16 v[120:123], v[188:191], a[144:147], v[120:123]\n"
    "v_mfma_f32_16x16x32_bf16 v[124:127], v[188:191], a[148:151], v[124:127]\n"
    "v_mfma_f32_16x16x32_bf16 v[128:131], v[188:191], a[152:155], v[128:131]\n"
    "v_mfma_f32_16x16x32_bf16 v[132:135], v[188:191], a[156:159], v[132:135]\n"
    "v_mfma_f32_16x16x32_bf16 v[120:123], v[192:195], a[160:163], v[120:123]\n"
    "s_sub_u32 s61, %[ss], 6\n"
    "s_and_b32 s61, s61, 7\n"
    "s_mul_i32 s61, s61, 9216\n"
    "s_mov_b64 s[62:63], s[54:55]\n"
    "s_add_u32 s62, s62, s61\n"
    "s_addc_u32 s63, s63, 0\n"
    "global_load_dwordx4 v[104:107], v17, s[62:63] sc1\n"
    "global_load_dwordx4 v[108:111], v17, s[62:63] offset:1024 sc1\n"
    "v_mfma_f32_16x16x32_bf16 v[124:127], v[192:195], a[164:167], v[124:127]\n"
    "v_mfma_f32_16x16x32_bf16 v[128:131], v[192:195], a[168:171], v[128:131]\n"
    "v_mfma_f32_16x16x32_bf16 v[132:135], v[192:195], a[172:175], v[132:135]\n"
    "v_mfma_f32_16x16x32_bf16 v[120:123], v[196:199], a[176:179], v[120:123]\n"
    "v_mfma_f32_16x16x32_bf16 v[124:127], v[196:199], a[180:183], v[124:127]\n"
    "v_mfma_f32_16x16x32_bf16 v[128:131], v[196:199], a[184:187], v[128:131]\n"
    "v_mfma_f32_16x16x32_bf16 v[132:135], v[196:199], a[188:191], v[132:135]\n"
    "v_mfma_f32_16x16x32_bf16 v[120:123], v[200:203], a[192:195], v[120:123]\n"
    "v_mfma_f32_16x16x32_bf16 v[124:127], v[200:203], a[196:199], v[124:127]\n"
    "v_mfma_f32_16x16x32_bf16 v[128:131], v[200:203], a[200:203], v[128:131]\n"
    "v_mfma_f32_16x16x32_bf16 v[132:135], v[200:203], a[204:207], v[132:135]\n"
    "v_mfma_f32_16x16x32_bf16 v[120:123], v[204:207], a[208:211], v[120:123]\n"
    "v_mfma_f32_16x16x32_bf16 v[124:127], v[204:207], a[212:215], v[124:127]\n"
    "v_mfma_f32_16x16x32_bf16 v[128:131], v[204:207], a[216:219], v[128:131]\n"
    "v_mfma_f32_16x16x32_bf16 v[132:135], v[204:207], a[220:223], v[132:135]\n"
    "s_waitcnt vmcnt(10)\n"
    "v_mfma_f32_16x16x32_bf16 v[120:123], v[208:211], a[224:227], v[120:123]\n"
    "v_mfma_f32_16x16x32_bf16 v[124:127], v[208:211], a[228:231], v[124:127]\n"
    "v_mfma_f32_16x16x32_bf16 v[128:131], v[208:211], a[232:235], v[128:131]\n"
    "v_mfma_f32_16x16x32_bf16 v[132:135], v[208:211], a[236:239], v[132:135]\n"
    "v_mfma_f32_16x16x32_bf16 v[120:123], v[212:215], a[240:243], v[120:123]\n"
    "v_mfma_f32_16x16x32_bf16 v[124:127], v[212:215], a[244:247], v[124:127]\n"
    "v_mfma_f32_16x16x32_bf16 v[128:131], v[212:215], a[248:251], v[128:131]\n"
    "v_mfma_f32_16x16x32_bf16 v[132:135], v[212:215], a[252:255], v[132:135]\n"
    "s_waitcnt vmcnt(8)\n"
    "v_mfma_f32_16x16x32_bf16 v[120:123], v[216:219], v[224:227], v[120:123]\n"
    "v_mfma_f32_16x16x32_bf16 v[124:127], v[216:219], v[228:231], v[124:127]\n"
    "v_mfma_f32_16x16x32_bf16 v[128:131], v[216:219], v[232:235], v[128:131]\n"
    "v_mfma_f32_16x16x32_bf16 v[132:135], v[216:219], v[236:239], v[132:135]\n"
    "v_mfma_f32_16x16x32_bf16 v[120:123], v[220:223], v[240:243], v[120:123]\n"
    "v_mfma_f32_16x16x32_bf16 v[124:127], v[220:223], v[244:247], v[124:127]\n"
    "v_mfma_f32_16x16x32_bf16 v[128:131], v[220:223], v[248:251], v[128:131]\n"
    "v_mfma_f32_16x16x32_bf16 v[132:135], v[220:223], v[252:255], v[132:135]\n"
    "s_sub_u32 s61, %[ss], 7\n"
    "s_and_b32 s61, s61, 7\n"
    "s_mul_i32 s61, s61, 9216\n"
    "s_mov_b64 s[62:63], s[54:55]\n"
    "s_add_u32 s62, s62, s61\n"
    "s_addc_u32 s63, s63, 0\n"
    "global_load_dwordx4 v[112:115], v17, s[62:63] sc1\n"
    "global_load_dwordx4 v[116:119], v17, s[62:63] offset:1024 sc1\n"
    "s_waitcnt vmcnt(4)\n"
    "v_mfma_f32_16x16x32_bf16 v[120:123], v[32:35], v[88:91], v[120:123]\n"
    "v_mfma_f32_16x16x32_bf16 v[124:127], v[36:39], v[88:91], v[124:127]\n"
    "v_mfma_f32_16x16x32_bf16 v[128:131], v[32:35], v[92:95], v[128:131]\n"
    "v_mfma_f32_16x16x32_bf16 v[132:135], v[36:39], v[92:95], v[132:135]\n"
    "v_mfma_f32_16x16x32_bf16 v[120:123], v[32:35], v[96:99], v[120:123]\n"
    "v_mfma_f32_16x16x32_bf16 v[124:127], v[36:39], v[96:99], v[124:127]\n"
    "v_mfma_f32_16x16x32_bf16 v[128:131], v[32:35], v[100:103], v[128:131]\n"
    "v_mfma_f32_16x16x32_bf16 v[132:135], v[36:39], v[100:103], v[132:135]\n"
    "s_waitcnt lgkmcnt(0)\n"
    "v_lshlrev_b32 v152, 16, v40\n"
    "v_and_b32 v153, 0xffff0000, v40\n"
    "v_lshlrev_b32 v154, 16, v41\n"
    "v_and_b32 v155, 0xffff0000, v41\n"
    "s_mov_b32 s46, 0x100000\n"
    "LCHK_%=:\n"
    "s_waitcnt vmcnt(0)\n"
    "v_max3_u32 v20, v104, v105, v106\n"
    "v_max3_u32 v21, v107, v108, v109\n"
    "v_max3_u32 v20, v20, v110, v111\n"
    "v_max3_u32 v21, v21, v112, v113\n"
    "v_max3_u32 v20, v20, v114, v115\n"
    "v_max3_u32 v21, v21, v116, v117\n"
    "v_max3_u32 v20, v20, v118, v119\n"
    "v_max_u32 v20, v20, v21\n"
    "v_cmp_eq_u32 vcc, -1, v20\n"
    "s_cbranch_vccz LOK_%=\n"
    "s_sub_u32 s46, s46, 1\n"
    "s_cmp_eq_u32 s46, 0\n"
    "s_cbranch_scc1 LOK_%=\n"
    "s_sub_u32 s61, %[ss], 6\n"
    "s_and_b32 s61, s61, 7\n"
    "s_mul_i32 s61, s61, 9216\n"
    "s_mov_b64 s[62:63], s[54:55]\n"
    "s_add_u32 s62, s62, s61\n"
    "s_addc_u32 s63, s63, 0\n"
    "global_load_dwordx4 v[104:107], v17, s[62:63] sc1\n"
    "global_load_dwordx4 v[108:111], v17, s[62:63] offset:1024 sc1\n"
    "s_sub_u32 s61, %[ss], 7\n"
    "s_and_b32 s61, s61, 7\n"
    "s_mul_i32 s61, s61, 9216\n"
    "s_mov_b64 s[62:63], s[54:55]\n"
    "s_add_u32 s62, s62, s61\n"
    "s_addc_u32 s63, s63, 0\n"
    "global_load_dwordx4 v[112:115], v17, s[62:63] sc1\n"
    "global_load_dwordx4 v[116:119], v17, s[62:63] offset:1024 sc1\n"
    "s_branch LCHK_%=\n"
    "LOK_%=:\n"
    "v_mfma_f32_16x16x32_bf16 v[120:123], v[32:35], v[104:107], v[120:123]\n"
    "v_mfma_f32_16x16x32_bf16 v[124:127], v[36:39], v[104:107], v[124:127]\n"
    "v_mfma_f32_16x16x32_bf16 v[128:131], v[32:35], v[108:111], v[128:131]\n"
    "v_mfma_f32_16x16x32_bf16 v[132:135], v[36:39], v[108:111], v[132:135]\n"
    "v_mfma_f32_16x16x32_bf16 v[120:123], v[32:35], v[112:115], v[120:123]\n"
    "v_mfma_f32_16x16x32_bf16 v[124:127], v[36:39], v[112:115], v[124:127]\n"
    "v_mfma_f32_16x16x32_bf16 v[128:131], v[32:35], v[116:119], v[128:131]\n"
    "v_mfma_f32_16x16x32_bf16 v[132:135], v[36:39], v[116:119], v[132:135]\n"
    "v_add_u32 v44, 0x4800, %[vout]\n"
    "v_add_u32 v45, 0x9000, %[vout]\n"
    "v_add_u32 v46, 0xd800, %[vout]\n"
    "s_nop 7\n"
    "s_nop 3\n"
    "v_cvt_pk_bf16_f32 v156, v120, v121\n"
    "v_lshlrev_b32 v157, 16, v156\n"
    "v_and_b32 v158, 0xffff0000, v156\n"
    "v_add_f32 v157, v157, v152\n"
    "v_add_f32 v158, v158, v153\n"
    "v_cvt_pk_bf16_f32 v156, v157, v158\n"
    "v_lshlrev_b32 v157, 16, v156\n"
    "v_and_b32 v158, 0xffff0000, v156\n"
    "v_lshlrev_b32 v159, 16, v48\n"
    "v_and_b32 v156, 0xffff0000, v48\n"
    "v_add_f32 v157, v157, v159\n"
    "v_add_f32 v158, v158, v156\n"
    "v_cvt_pk_bf16_f32 v56, v157, v158\n"
    "v_cvt_pk_bf16_f32 v156, v122, v123\n"
    "v_lshlrev_b32 v157, 16, v156\n"
    "v_and_b32 v158, 0xffff0000, v156\n"
    "v_add_f32 v157, v157, v154\n"
    "v_add_f32 v158, v158, v155\n"
    "v_cvt_pk_bf16_f32 v156, v157, v158\n"
    "v_lshlrev_b32 v157, 16, v156\n"
    "v_and_b32 v158, 0xffff0000, v156\n"
    "v_lshlrev_b32 v159, 16, v49\n"
    "v_and_b32 v156, 0xffff0000, v49\n"
    "v_add_f32 v157, v157, v159\n"
    "v_add_f32 v158, v158, v156\n"
    "v_cvt_pk_bf16_f32 v57, v157, v158\n"
    "global_store_dwordx2 %[vout], v[56:57], s[56:57]\n"
    "v_cvt_pk_bf16_f32 v156, v124, v125\n"
    "v_lshlrev_b32 v157, 16, v156\n"
    "v_and_b32 v158, 0xffff0000, v156\n"
    "v_add_f32 v157, v157, v152\n"
    "v_add_f32 v158, v158, v153\n"
    "v_cvt_pk_bf16_f32 v156, v157, v158\n"
    "v_lshlrev_b32 v157, 16, v156\n"
    "v_and_b32 v158, 0xffff0000, v156\n"
    "v_lshlrev_b32 v159, 16, v50\n"
    "v_and_b32 v156, 0xffff0000, v50\n"
    "v_add_f32 v157, v157, v159\n"
    "v_add_f32 v158, v158, v156\n"
    "v_cvt_pk_bf16_f32 v58, v157, v158\n"
    "v_cvt_pk_bf16_f32 v156, v126, v127\n"
    "v_lshlrev_b32 v157, 16, v156\n"
    "v_and_b32 v158, 0xffff0000, v156\n"
    "v_add_f32 v157, v157, v154\n"
    "v_add_f32 v158, v158, v155\n"
    "v_cvt_pk_bf16_f32 v156, v157, v158\n"
    "v_lshlrev_b32 v157, 16, v156\n"
    "v_and_b32 v158, 0xffff0000, v156\n"
    "v_lshlrev_b32 v159, 16, v51\n"
    "v_and_b32 v156, 0xffff0000, v51\n"
    "v_add_f32 v157, v157, v159\n"
    "v_add_f32 v158, v158, v156\n"
    "v_cvt_pk_bf16_f32 v59, v157, v158\n"
    "global_store_dwordx2 v44, v[58:59], s[56:57]\n"
    "v_cvt_pk_bf16_f32 v156, v128, v129\n"
    "v_lshlrev_b32 v157, 16, v156\n"
    "v_and_b32 v158, 0xffff0000, v156\n"
    "v_add_f32 v157, v157, v152\n"
    "v_add_f32 v158, v158, v153\n"
    "v_cvt_pk_bf16_f32 v156, v157, v158\n"
    "v_lshlrev_b32 v157, 16, v156\n"
    "v_and_b32 v158, 0xffff0000, v156\n"
    "v_lshlrev_b32 v159, 16, v52\n"
    "v_and_b32 v156, 0xffff0000, v52\n"
    "v_add_f32 v157, v157, v159\n"
    "v_add_f32 v158, v158, v156\n"
    "v_cvt_pk_bf16_f32 v60, v157, v158\n"
    "v_cvt_pk_bf16_f32 v156, v130, v131\n"
    "v_lshlrev_b32 v157, 16, v156\n"
    "v_and_b32 v158, 0xffff0000, v156\n"
    "v_add_f32 v157, v157, v154\n"
    "v_add_f32 v158, v158, v155\n"
    "v_cvt_pk_bf16_f32 v156, v157, v158\n"
    "v_lshlrev_b32 v157, 16, v156\n"
    "v_and_b32 v158, 0xffff0000, v156\n"
    "v_lshlrev_b32 v159, 16, v53\n"
    "v_and_b32 v156, 0xffff0000, v53\n"
    "v_add_f32 v157, v157, v159\n"
    "v_add_f32 v158, v158, v156\n"
    "v_cvt_pk_bf16_f32 v61, v157, v158\n"
    "global_store_dwordx2 v45, v[60:61], s[56:57]\n"
    "v_cvt_pk_bf16_f32 v156, v132, v133\n"
    "v_lshlrev_b32 v157, 16, v156\n"
    "v_and_b32 v158, 0xffff0000, v156\n"
    "v_add_f32 v157, v157, v152\n"
    "v_add_f32 v158, v158, v153\n"
    "v_cvt_pk_bf16_f32 v156, v157, v158\n"
    "v_lshlrev_b32 v157, 16, v156\n"
    "v_and_b32 v158, 0xffff0000, v156\n"
    "v_lshlrev_b32 v159, 16, v54\n"
    "v_and_b32 v156, 0xffff0000, v54\n"
    "v_add_f32 v157, v157, v159\n"
    "v_add_f32 v158, v158, v156\n"
    "v_cvt_pk_bf16_f32 v62, v157, v158\n"
    "v_cvt_pk_bf16_f32 v156, v134, v135\n"
    "v_lshlrev_b32 v157, 16, v156\n"
    "v_and_b32 v158, 0xffff0000, v156\n"
    "v_add_f32 v157, v157, v154\n"
    "v_add_f32 v158, v158, v155\n"
    "v_cvt_pk_bf16_f32 v156, v157, v158\n"
    "v_lshlrev_b32 v157, 16, v156\n"
    "v_and_b32 v158, 0xffff0000, v156\n"
    "v_lshlrev_b32 v159, 16, v55\n"
    "v_and_b32 v156, 0xffff0000, v55\n"
    "v_add_f32 v157, v157, v159\n"
    "v_add_f32 v158, v158, v156\n"
    "v_cvt_pk_bf16_f32 v63, v157, v158\n"
    "global_store_dwordx2 v46, v[62:63], s[56:57]\n"
    "K3END_%=:\n"
    "K3_CODE_E:\n"
    :
    : [vw0] "v"(vw0), [vg0] "v"(vg0), [vt0] "v"(vt0), [vhw] "v"(vhw), [vhr] "v"(vhr), [vp] "v"(vp), [vsr] "v"(vsr),
      [vsw] "v"(vsw), [vpo] "v"(vpo), [vcl] "v"(vcl), [vout] "v"(vout), [vb2] "v"(vb2), [vsx] "v"(vsx), [vsy] "v"(vsy),
      [vho] "v"(vho),
      [sw] "s"(sw), [sht] "s"(sht), [spart] "s"(spart), [scs] "s"(scs), [sb1] "s"(sb1), [sm0] "s"(sm0), [swx] "s"(swx),
      [sfl] "s"(sfl), [sown] "s"(sown), [sout] "s"(sout), [sb2] "s"(sb2), [sfe] "s"(sfe), [swv] "s"(swv),
      [sbase] "s"(sbase), [srot] "s"(srot), [ss] "s"(ss), [sxoff] "s"(sxoff), [sdx] "s"(sdx), [shx] "s"(shx)
    : "v16", "v17", "v18", "v19", "v20", "v21", "v22", "v23", "v24", "v25", "v26", "v27", "v28", "v29", "v30", "v31", "v32", "v33", "v34", "v35", "v36", "v37", "v38", "v39", "v40", "v41", "v42", "v43", "v44", "v45", "v46", "v47", "v48", "v49", "v50", "v51", "v52", "v53", "v54", "v55", "v56", "v57", "v58", "v59", "v60", "v61", "v62", "v63", "v64", "v65", "v66", "v67", "v68", "v69", "v70", "v71", "v72", "v73", "v74", "v75", "v76", "v77", "v78", "v79", "v80", "v81", "v82", "v83", "v84", "v85", "v86", "v87", "v88", "v89", "v90", "v91", "v92", "v93", "v94", "v95", "v96", "v97", "v98", "v99", "v100", "v101", "v102", "v103", "v104", "v105", "v106", "v107", "v108", "v109", "v110", "v111", "v112", "v113", "v114", "v115", "v116", "v117", "v118", "v119", "v120", "v121", "v122", "v123", "v124", "v125", "v126", "v127", "v128", "v129", "v130", "v131", "v132", "v133", "v134", "v135", "v136", "v137", "v138", "v139", "v140", "v141", "v142", "v143", "v144", "v145", "v146", "v147", "v148", "v149", "v150", "v151", "v152", "v153", "v154", "v155", "v156", "v157", "v158", "v159", "v160", "v161", "v162", "v163", "v164", "v165", "v166", "v167", "v168", "v169", "v170", "v171", "v172", "v173", "v174", "v175", "v176", "v177", "v178", "v179", "v180", "v181", "v182", "v183", "v184", "v185", "v186", "v187", "v188", "v189", "v190", "v191", "v192", "v193", "v194", "v195", "v196", "v197", "v198", "v199", "v200", "v201", "v202", "v203", "v204", "v205", "v206", "v207", "v208", "v209", "v210", "v211", "v212", "v213", "v214", "v215", "v216", "v217", "v218", "v219", "v220", "v221", "v222", "v223", "v224", "v225", "v226", "v227", "v228", "v229", "v230", "v231", "v232", "v233", "v234", "v235", "v236", "v237", "v238", "v239", "v240", "v241", "v242", "v243", "v244", "v245", "v246", "v247", "v248", "v249", "v250", "v251", "v252", "v253", "v254", "v255", "a0", "a1", "a2", "a3", "a4", "a5", "a6", "a7", "a8", "a9", "a10", "a11", "a12", "a13", "a14", "a15", "a16", "a17", "a18", "a19", "a20", "a21", "a22", "a23", "a24", "a25", "a26", "a27", "a28", "a29", "a30", "a31", "a32", "a33", "a34", "a35", "a36", "a37", "a38", "a39", "a40", "a41", "a42", "a43", "a44", "a45", "a46", "a47", "a48", "a49", "a50", "a51", "a52", "a53", "a54", "a55", "a56", "a57", "a58", "a59", "a60", "a61", "a62", "a63", "a64", "a65", "a66", "a67", "a68", "a69", "a70", "a71", "a72", "a73", "a74", "a75", "a76", "a77", "a78", "a79", "a80", "a81", "a82", "a83", "a84", "a85", "a86", "a87", "a88", "a89", "a90", "a91", "a92", "a93", "a94", "a95", "a96", "a97", "a98", "a99", "a100", "a101", "a102", "a103", "a104", "a105", "a106", "a107", "a108", "a109", "a110", "a111", "a112", "a113", "a114", "a115", "a116", "a117", "a118", "a119", "a120", "a121", "a122", "a123", "a124", "a125", "a126", "a127", "a128", "a129", "a130", "a131", "a132", "a133", "a134", "a135", "a136", "a137", "a138", "a139", "a140", "a141", "a142", "a143", "a144", "a145", "a146", "a147", "a148", "a149", "a150", "a151", "a152", "a153", "a154", "a155", "a156", "a157", "a158", "a159", "a160", "a161", "a162", "a163", "a164", "a165", "a166", "a167", "a168", "a169", "a170", "a171", "a172", "a173", "a174", "a175", "a176", "a177", "a178", "a179", "a180", "a181", "a182", "a183", "a184", "a185", "a186", "a187", "a188", "a189", "a190", "a191", "a192", "a193", "a194", "a195", "a196", "a197", "a198", "a199", "a200", "a201", "a202", "a203", "a204", "a205", "a206", "a207", "a208", "a209", "a210", "a211", "a212", "a213", "a214", "a215", "a216", "a217", "a218", "a219", "a220", "a221", "a222", "a223", "a224", "a225", "a226", "a227", "a228", "a229", "a230", "a231", "a232", "a233", "a234", "a235", "a236", "a237", "a238", "a239", "a240", "a241", "a242", "a243", "a244", "a245", "a246", "a247", "a248", "a249", "a250", "a251", "a252", "a253", "a254", "a255", "s36", "s37", "s38", "s39", "s40", "s41", "s42", "s43", "s44", "s45", "s46", "s47", "s48", "s49", "s50", "s51", "s52", "s53", "s54", "s55", "s56", "s57", "s58", "s59", "s60", "s61", "s62", "s63", "s64", "s65", "m0", "scc", "vcc", "memory"
  );
  TL_END(2)
}
#define K2PF 0
#define K1PF 1
typedef unsigned short u16;
typedef _Float16 h16x2 __attribute__((ext_vector_type(2)));
__device__ __forceinline__ float bf2f(u16 v) { return __builtin_bit_cast(float, (u32)v << 16); }
__device__ __forceinline__ u32 cvt_pk_f16(float a, float b) {
  f32x2 v = {a, b};
  return __builtin_bit_cast(u32, __builtin_convertvector(v, h16x2));
}
__device__ __forceinline__ float h_lo(u32 x) { return (float)__builtin_bit_cast(h16x2, x)[0]; }
__device__ __forceinline__ float h_hi(u32 x) { return (float)__builtin_bit_cast(h16x2, x)[1]; }
template <class T> __device__ __forceinline__ T lds_ld(u32 a) { return *(const LAS T*)a; }
template <class T> __device__ __forceinline__ void lds_st(u32 a, T v) { *(LAS T*)a = v; }

#define LN_MID(i)
__device__ __forceinline__ void ln_job(int jb, const u16* __restrict__ x, char* __restrict__ ws) {
  const int tid = threadIdx.x, lane = tid & 63, w = __builtin_amdgcn_readfirstlane(tid >> 6);
  const int jt = jb >> 1, hf = jb & 1;
  const int p = lane & 7, r = lane >> 3;
  u32x4 v[2][9];
#pragma unroll
  for (int ps = 0; ps < 2; ++ps) {
    const u32x4* src = (const u32x4*)(x + (size_t)(64 * jt + 16 * w + 8 * ps + r) * D) + p;
#pragma unroll
    for (int ii = 0; ii < 5; ++ii) v[ps][ii] = src[8 * (hf ? 8 - ii : ii)];
#pragma unroll
    for (int k = 0; k < 4; ++k) v[ps][5 + k] = src[8 * (hf ? k : 5 + k)];
  }
  LN_MID(0)
  const bool own4 = (p < 4) == (hf == 0);
#pragma unroll
  for (int ps = 0; ps < 2; ++ps)
#pragma unroll
    for (int ii = 0; ii < 5; ++ii) {
      const u32 j = p + 8 * (hf ? 8 - ii : ii) - 36 * hf;
      const u32 a = (16 * w + 8 * ps + r) * 576 + (((j >> 1) ^ ps) * 32) + (j & 1) * 16;
      if (ii < 4 || own4) lds_st<u32x4>(a, v[ps][ii]);
    }
  f32x2 stat[2];
#pragma unroll
  for (int ps = 0; ps < 2; ++ps) {
    float s = 0.f, q2 = 0.f;
#pragma unroll
    for (int i = 0; i < 9; ++i)
#pragma unroll
      for (int k = 0; k < 4; ++k) { s = dot2bf(v[ps][i][k], 0x3f803f80u, s); q2 = dot2bf(v[ps][i][k], v[ps][i][k], q2); }
    const float mean = sum8(s) * (1.f / D);
    const float var = fmaxf(fmaf(-mean, mean, sum8(q2) * (1.f / D)), 0.f);
    const float m = rbf(mean);
    const float rs = __builtin_amdgcn_rcpf(rbf(__builtin_amdgcn_sqrtf(rbf(rbf(var) + 1e-5f))));
    stat[ps] = f32x2{m, rs};
  }
  asm volatile("" : "+v"(stat[0]), "+v"(stat[1]));
  if (hf == 0 && p == 0) {
#pragma unroll
    for (int ps = 0; ps < 2; ++ps) *(f32x2*)(ws + WS_ST + (size_t)(64 * jt + 16 * w + 8 * ps + r) * 8) = stat[ps];
  }
  LN_MID(2)
  __syncthreads();
  LN_MID(3)
  const int q = lane >> 4, c = lane & 15, aa = (lane >> 2) & 3, pp = lane & 3;
  const u32 ta = (8 * q + aa) * 576 + pp * 8;
  const u32 sw = 36864 + w * 9216 + c * 128, sr = 36864 + w * 9216 + (lane >> 3) * 128 + (((lane & 7) ^ (lane >> 3)) * 16);
  char* dst = ws + WS_XT + (size_t)(288 * hf + (lane >> 3)) * T_ROW + (64 * jt + 8 * (lane & 7)) * 2;
  u32x4 fr[5][2];
#pragma unroll
  for (int i = 0; i < 5; ++i) {
    const int tile = (w + 4 * i) < 18 ? w + 4 * i : 17;
    const u32 a = ta + ((tile ^ (q & 1)) * 32);
#pragma unroll
    for (int h = 0; h < 2; ++h) {
      const u32x2 lo = tr_read(a + h * 32 * 576), hi = tr_read(a + h * 32 * 576 + 4 * 576);
      fr[i][h] = u32x4{lo[0], lo[1], hi[0], hi[1]};
    }
  }
#pragma unroll
  for (int i = 0; i < 4; ++i) {
    lds_st<u32x4>(sw + i * 2048 + ((q ^ (c & 7)) * 16), fr[i][0]);
    lds_st<u32x4>(sw + i * 2048 + (((4 + q) ^ (c & 7)) * 16), fr[i][1]);
  }
#pragma unroll
  for (int i = 0; i < 4; ++i) { fr[i][0] = lds_ld<u32x4>(sr + i * 2048); fr[i][1] = lds_ld<u32x4>(sr + i * 2048 + 1024); }
  if (w < 2) {
    lds_st<u32x4>(sw + ((q ^ (c & 7)) * 16), fr[4][0]);
    lds_st<u32x4>(sw + (((4 + q) ^ (c & 7)) * 16), fr[4][1]);
    fr[4][0] = lds_ld<u32x4>(sr); fr[4][1] = lds_ld<u32x4>(sr + 1024);
  }
#pragma unroll
  for (int i = 0; i < 4; ++i) {
    st16_wt(dst + (size_t)(w + 4 * i) * 16 * T_ROW, fr[i][0]);
    st16_wt(dst + (size_t)((w + 4 * i) * 16 + 8) * T_ROW, fr[i][1]);
  }
  if (w < 2) {
    st16_wt(dst + (size_t)(w + 16) * 16 * T_ROW, fr[4][0]);
    st16_wt(dst + (size_t)((w + 16) * 16 + 8) * T_ROW, fr[4][1]);
  }
  LN_MID(4)
  LABEL("LN_CODE_E")
}

__device__ __forceinline__ void cmul4(f32x2& a0, f32x2& a1, f32x2& a2, f32x2& a3, f32x2 w0, f32x2 w1, f32x2 w2, f32x2 w3) {
  f32x2 t0, t1, t2, t3;
  asm("v_pk_mul_f32 %4, %0, %8 op_sel:[0,0] op_sel_hi:[0,1]\n\t"
      "v_pk_mul_f32 %5, %1, %9 op_sel:[0,0] op_sel_hi:[0,1]\n\t"
      "v_pk_mul_f32 %6, %2, %10 op_sel:[0,0] op_sel_hi:[0,1]\n\t"
      "v_pk_mul_f32 %7, %3, %11 op_sel:[0,0] op_sel_hi:[0,1]\n\t"
      "v_pk_fma_f32 %0, %0, %8, %4 op_sel:[1,1,0] op_sel_hi:[1,0,1] neg_lo:[1,0,0]\n\t"
      "v_pk_fma_f32 %1, %1, %9, %5 op_sel:[1,1,0] op_sel_hi:[1,0,1] neg_lo:[1,0,0]\n\t"
      "v_pk_fma_f32 %2, %2, %10, %6 op_sel:[1,1,0] op_sel_hi:[1,0,1] neg_lo:[1,0,0]\n\t"
      "v_pk_fma_f32 %3, %3, %11, %7 op_sel:[1,1,0] op_sel_hi:[1,0,1] neg_lo:[1,0,0]"
      : "+v"(a0), "+v"(a1), "+v"(a2), "+v"(a3), "=&v"(t0), "=&v"(t1), "=&v"(t2), "=&v"(t3) : "v"(w0), "v"(w1), "v"(w2), "v"(w3));
}
__device__ __forceinline__ f32x2 cmul(f32x2 a, f32x2 b) {
  f32x2 t;
  asm("v_pk_mul_f32 %1, %0, %2 op_sel:[0,0] op_sel_hi:[0,1]\n\t"
      "v_pk_fma_f32 %0, %0, %2, %1 op_sel:[1,1,0] op_sel_hi:[1,0,1] neg_lo:[1,0,0]" : "+v"(a), "=&v"(t) : "v"(b));
  return a;
}
__device__ __forceinline__ f32x2 cexpp(float zr2, float zrev, float p) {
  const float m = __builtin_amdgcn_exp2f(zr2 * p), f = __builtin_amdgcn_fractf(zrev * p);
  return f32x2{m * __builtin_amdgcn_cosf(f), m * __builtin_amdgcn_sinf(f)};
}
__device__ __forceinline__ void st_row4(u32 a, f32x2 x0, f32x2 x1, f32x2 x2, f32x2 x3, int rs) {
  lds_st<u32>(a, cvt_pk_f16(x0[0], x0[1])); lds_st<u32>(a + rs, cvt_pk_f16(x1[0], x1[1]));
  lds_st<u32>(a + 2 * rs, cvt_pk_f16(x2[0], x2[1])); lds_st<u32>(a + 3 * rs, cvt_pk_f16(x3[0], x3[1]));
}
#define KG_MID(i)
constexpr int KG_RS = 544, KG_CH = 64 * KG_RS, KG_KT = 2 * KG_CH, KG_KTS = 1552, KG_RED = KG_KT + 2 * KG_KTS;

__device__ __forceinline__ void kgen_job(int jb, const u16* __restrict__ lar, const u16* __restrict__ aim, const u16* __restrict__ cre,
                                         const u16* __restrict__ cim, const u16* __restrict__ ldt,
                                         const u16* __restrict__ w1, const u16* __restrict__ w2, char* __restrict__ ws) {
  const int tid = threadIdx.x, lane = tid & 63, w = __builtin_amdgcn_readfirstlane(tid >> 6);
  const int cc = w >> 1, n = tid & 127, d = 2 * jb + cc;
  const u16 raw_dt = ldt[d], raw_lar = lar[d * NM + n], raw_aim = aim[d * NM + n], raw_cre = cre[d * NM + n], raw_cim = cim[d * NM + n];
  u32 ix[8];
#pragma unroll
  for (int j = 0; j < 8; j += 2) {
    const int l = 2047 - 8 * tid - j;
    const u32 pk = cvt_pk_bf16((float)l, (float)(l - 1));
    const u32 p0 = (u32)bf_lo(pk), p1 = (u32)bf_hi(pk);
    ix[j] = 2 * min(p0, min((p0 >> 2) + 384, (p0 >> 3) + 512));
    ix[j + 1] = 2 * min(p1, min((p1 >> 2) + 384, (p1 >> 3) + 512));
  }
#pragma unroll
  for (int j = 0; j < 8; ++j) asm volatile("" : "+v"(ix[j]));
  const float dt = rbf(expf(bf2f(raw_dt)));
  const float zr = -rbf(expf(bf2f(raw_lar))) * dt, zi = bf2f(raw_aim) * dt;
  const float cr = bf2f(raw_cre) * dt, ci = bf2f(raw_cim) * dt;
  float sa = fabsf(cr) + fabsf(ci), sr = cr;
  sa = sum64(sa); sr = sum64(sr);
  if (lane == 0) lds_st<f32x2>(KG_RED + w * 8, f32x2{sa, sr});
  const float zr2 = zr * 1.44269504088896341f, zrev = zi * 0.15915494309189535f;
  const u32 tb = cc * KG_CH + n * 4;
  u32 pf2, pf3;
  int tq = tid;
  asm volatile("" : "+v"(tq) : "v"(zr2), "v"(zrev), "v"(cr), "v"(ci));
  u32 pf;
  CODE_TOUCH(pf, "KG_PC", "KG_PC", "KG_CODE_E", tq)
  CODE_TOUCH(pf2, "KG_PC2", "K2_CODE_B", "K2_CODE_E", tq)
  CODE_TOUCH(pf3, "KG_PC3", "K3_CODE_B", "K3_CODE_E", tq)
  {
    const f32x2 w1 = cexpp(zr2, zrev, 1.f);
    f32x2 b0 = w1, b1 = cmul(w1, w1), b2 = cmul(b1, w1), b3 = cmul(b1, b1);
    const f32x2 w4 = b3;
    st_row4(tb, b0, b1, b2, b3, KG_RS);
    cmul4(b0, b1, b2, b3, w4, w4, w4, w4);
    st_row4(tb + 4 * KG_RS, b0, b1, b2, b3, KG_RS);
    const f32x2 w8 = b3;
    cmul4(b0, b1, b2, b3, w4, w4, w4, w4);
    st_row4(tb + 8 * KG_RS, b0, b1, b2, b3, KG_RS);
    const f32x2 w12 = b3;
    cmul4(b0, b1, b2, b3, w4, w4, w4, w4);
    st_row4(tb + 12 * KG_RS, b0, b1, b2, b3, KG_RS);
    const f32x2 e256 = cexpp(zr2, zrev, 256.f), e512 = cexpp(zr2, zrev, 512.f), g0 = cexpp(zr2, zrev, 516.f), h0 = cexpp(zr2, zrev, 1032.f);
    b0 = g0; b1 = g0; b2 = g0; b3 = h0;
    cmul4(b0, b1, b2, b3, w4, w8, w12, w8);
    st_row4(tb + 48 * KG_RS, g0, b0, b1, b2, KG_RS);
    f32x2 c0 = g0, h1 = b3;
    cmul4(c0, b0, b1, b2, e256, e256, e256, e256);
    st_row4(tb + 52 * KG_RS, c0, b0, b1, b2, KG_RS);
    b0 = h0; b1 = h1; b2 = cmul(h0, e256); b3 = cmul(h1, e256);
    st_row4(tb + 56 * KG_RS, b0, b1, b2, b3, KG_RS);
    cmul4(b0, b1, b2, b3, e512, e512, e512, e512);
    st_row4(tb + 60 * KG_RS, b0, b1, b2, b3, KG_RS);
  }
  KG_MID(0)
  __syncthreads();
  const f32x2 r0 = lds_ld<f32x2>(KG_RED + cc * 16), r1 = lds_ld<f32x2>(KG_RED + cc * 16 + 8);
  int ex = __builtin_amdgcn_frexp_expf(2.f * (r0[0] + r1[0]));
  ex = ex < -100 ? -100 : (ex > 100 ? 100 : ex);
  const float sc = __builtin_amdgcn_ldexpf(1.f, 14 - ex);
  {
    const f32x2 w16 = cexpp(zr2, -zrev, 16.f), w64 = cexpp(zr2, -zrev, 64.f);
    f32x2 a0 = {2.f * sc * cr, -2.f * sc * ci};
    f32x2 a1 = cmul(a0, w16), a2 = cmul(a1, w16), a3 = cmul(a2, w16);
#pragma unroll
    for (int k = 0; k < 8; ++k) {
      st_row4(tb + (16 + 4 * k) * KG_RS, a0, a1, a2, a3, KG_RS);
      if (k < 7) cmul4(a0, a1, a2, a3, w64, w64, w64, w64);
    }
  }
  KG_MID(1)
  __syncthreads();
  const int q = lane >> 4, c = lane & 15;
  const u32 fa = cc * KG_CH + c * KG_RS + q * 16, kt = KG_KT + cc * KG_KTS;
  if ((w & 1) == 0) {
    f32x4 a0 = {0, 0, 0, 0}, a1 = {0, 0, 0, 0};
#pragma unroll
    for (int s = 0; s < 8; ++s) {
      const f16x8 b = lds_ld<f16x8>(fa + s * 64);
      a0 = __builtin_amdgcn_mfma_f32_16x16x32_f16(lds_ld<f16x8>(fa + 16 * KG_RS + s * 64), b, a0, 0, 0, 0);
      a1 = __builtin_amdgcn_mfma_f32_16x16x32_f16(lds_ld<f16x8>(fa + 32 * KG_RS + s * 64), b, a1, 0, 0, 0);
    }
#pragma unroll
    for (int i = 0; i < 4; ++i) {
      lds_st<_Float16>(kt + 2 * (1 + c + 16 * (4 * q + i)), (_Float16)a0[i]);
      lds_st<_Float16>(kt + 2 * (257 + c + 16 * (4 * q + i)), (_Float16)a1[i]);
    }
  } else {
    f32x4 a0 = {0, 0, 0, 0};
#pragma unroll
    for (int s = 0; s < 8; ++s)
      a0 = __builtin_amdgcn_mfma_f32_16x16x32_f16(lds_ld<f16x8>(fa + 16 * KG_RS + s * 64), lds_ld<f16x8>(fa + 48 * KG_RS + s * 64), a0, 0, 0, 0);
    const int i0 = c < 8 ? 513 + (c & 3) + 64 * (c >> 2) : 641 + (c & 1) + 32 * ((c - 8) >> 1), st = c < 8 ? 4 : 2;
#pragma unroll
    for (int i = 0; i < 4; ++i) lds_st<_Float16>(kt + 2 * (i0 + st * (4 * q + i)), (_Float16)a0[i]);
    if (lane == 0) {
      lds_st<_Float16>(kt, (_Float16)(2.f * sc * (r0[1] + r1[1])));
      ((float*)(ws + WS_GS))[d] = __builtin_amdgcn_ldexpf(1.f, ex - 14);
    }
  }
  KG_MID(2)
  __syncthreads();
#pragma unroll
  for (int ch = 0; ch < 2; ++ch) {
    u32x4 o;
#pragma unroll
    for (int j = 0; j < 4; ++j)
      o[j] = (u32)lds_ld<u16>(KG_KT + ch * KG_KTS + ix[2 * j]) | ((u32)lds_ld<u16>(KG_KT + ch * KG_KTS + ix[2 * j + 1]) << 16);
    st16_wt(ws + WS_G + (size_t)(2 * jb + ch) * G_ROW + 64 + 16 * tid, o);
  }
  if (tid < 32) {
    const int k = tid & 15;
    st16_wt(ws + WS_G + (size_t)(2 * jb + (tid >> 4)) * G_ROW + (k < 4 ? 16 * k : 4096 + 16 * k), u32x4{0, 0, 0, 0});
  }
  asm volatile("s_waitcnt vmcnt(0)" :: "v"(pf), "v"(pf2), "v"(pf3));
  LABEL("KG_CODE_E")
}

__device__ __forceinline__ void pretile_job(int j, const u16* __restrict__ w1, const u16* __restrict__ w2, char* __restrict__ ws) {
  if (j < 72) {
    const int s = j / 9, cb = j % 9;
    pretile<true>((const char*)w1 + (size_t)(s * 576 + cb * 64) * 2, F * 2, ws + WS_WT, s, cb, 0, (float*)(ws + WS_CS));
  } else {
    const int s = (j - 72) / 9, cb = (j - 72) % 9;
    pretile<false>((const char*)w2 + ((size_t)s * 576 * D + cb * 64) * 2, D * 2, ws + WS_WT, s, cb, 1, nullptr);
  }
}
#define K1_NW 0
constexpr int K1_N_LN = 64, K1_N_KG = 288, K1_GRID = K1_N_LN + K1_NW + K1_N_KG;
constexpr int K1_LDS = 73728;

__global__ __launch_bounds__(256, 2)
void k1_pre(const u16* __restrict__ x, const u16* __restrict__ lar, const u16* __restrict__ aim, const u16* __restrict__ cre,
            const u16* __restrict__ cim, const u16* __restrict__ ldt, const u16* __restrict__ w1, const u16* __restrict__ w2,
            char* __restrict__ ws) {
  TL_BEGIN
  int b = blockIdx.x;
  if (b < 4) *(u32x4*)(ws + WS_CNT + (b * 256 + threadIdx.x) * 128) = u32x4{0, 0, 0, 0};
  if (b < K1_N_KG) { kgen_job(b, lar, aim, cre, cim, ldt, w1, w2, ws); TL_END(0) return; }
  b -= K1_N_KG;
  if (b < K1_N_LN) { ln_job(b, x, ws); TL_END(0) return; }
  b -= K1_N_LN;
  pretile_job(b, w1, w2, ws);
  TL_END(0)
}

constexpr int C2_GB = 128, C2_CS = 4416, C2_GCH = 4 * C2_CS, C2_UB = C2_GB + 3 * C2_GCH, C2_UCH = 96 * 80, K2_LDS = C2_UB + 3 * C2_UCH;
constexpr int C2_YCH = 96 * 144;

__device__ __forceinline__ void g_copies(u32 base, int M, u32x4 a, u32x2 b) {
  const u32 e0 = __builtin_amdgcn_alignbit(a[1], a[0], 16), e1 = __builtin_amdgcn_alignbit(a[2], a[1], 16);
  const u32 e2 = __builtin_amdgcn_alignbit(a[3], a[2], 16), e3 = __builtin_amdgcn_alignbit(b[0], a[3], 16);
  const u32 e4 = __builtin_amdgcn_alignbit(b[1], b[0], 16);
  const u32 p = base + M * 16;
  lds_st<u32x4>(p, a);
  lds_st<u32x4>(p + C2_CS, u32x4{e0, e1, e2, e3});
  lds_st<u32x4>(p + 2 * C2_CS, u32x4{a[1], a[2], a[3], b[0]});
  lds_st<u32x4>(p + 3 * C2_CS, u32x4{e1, e2, e3, e4});
}
constexpr int K2_NW = 144 - K1_NW, K2_GRID = 192 + K2_NW;

__global__ __launch_bounds__(256, 2)
void k2_conv(const u16* __restrict__ dskip, const u16* __restrict__ w1, const u16* __restrict__ w2, const char* __restrict__ ws_c, char* __restrict__ ws) {
  TL_BEGIN
  const int tid = threadIdx.x, lane = tid & 63, w = __builtin_amdgcn_readfirstlane(tid >> 6);
  LABEL("K2_CODE_B")
  if (blockIdx.x >= 192) {
    u32 pfw;
    CODE_TOUCH(pfw, "K2W_PC", "K2W_PC", "K2W_CODE_E", tid)
    pretile_job(blockIdx.x - 192 + K1_NW, w1, w2, ws);
    LABEL("K2W_CODE_E")
    asm volatile("s_waitcnt vmcnt(0)" :: "v"(pfw));
    TL_END(1)
    return;
  }
  u32 pf, pf3;
  CODE_TOUCH(pf, "K2_PC", "K2_PC", "K2_CODE_E", tid)
  CODE_TOUCH(pf3, "K2_PC3", "K3_CODE_B", "K3_CODE_E", tid)
  const int d0 = 3 * blockIdx.x;
  u32x4 ga[3], uv[3], xv[3]; u32x2 gb[3];
  f32x4 st[4];
#pragma unroll
  for (int k = 0; k < 4; ++k) st[k] = *(const f32x4*)(ws_c + WS_ST + 64 * tid + 16 * k);
#pragma unroll
  for (int c = 0; c < 3; ++c) xv[c] = *(const u32x4*)(ws_c + WS_XT + (size_t)(d0 + c) * T_ROW + 16 * tid);
#pragma unroll
  for (int c = 0; c < 3; ++c) {
    const char* g = ws_c + WS_G + (size_t)(d0 + c) * G_ROW + 64 + 16 * tid;
    ga[c] = *(const u32x4*)g; gb[c] = *(const u32x2*)(g + 16);
  }
  u32x4 ea = {0, 0, 0, 0}; u32x2 eb = {0, 0};
  const int ec = tid >> 3, eM = (tid & 7) < 4 ? (tid & 7) : 256 + (tid & 7);
  if (tid < 24) {
    const char* g = ws_c + WS_G + (size_t)(d0 + ec) * G_ROW + 16 * eM;
    ea = *(const u32x4*)g; eb = *(const u32x2*)(g + 16);
  }
  float isc[3], dsk[3];
#pragma unroll
  for (int c = 0; c < 3; ++c) { isc[c] = ((const float*)(ws_c + WS_GS))[d0 + c]; dsk[c] = bf2f(dskip[d0 + c]); }
  if (tid < 128) {
#pragma unroll
    for (int c = 0; c < 3; ++c) lds_st<u32x4>(C2_UB + c * C2_UCH + (tid >> 2) * 80 + (tid & 3) * 16, u32x4{0, 0, 0, 0});
  }
#pragma unroll
  for (int c = 0; c < 3; ++c) {
    u32x4 uh;
#pragma unroll
    for (int k = 0; k < 4; ++k) {
      const u32 t = cvt_pk_bf16(bf_lo(xv[c][k]) - st[k][0], bf_hi(xv[c][k]) - st[k][2]);
      uv[c][k] = cvt_pk_bf16(bf_lo(t) * st[k][1], bf_hi(t) * st[k][3]);
      uh[k] = cvt_pk_f16(bf_lo(uv[c][k]), bf_hi(uv[c][k]));
    }
    lds_st<u32x4>(C2_UB + c * C2_UCH + (32 + (tid >> 2)) * 80 + (tid & 3) * 16, uh);
  }
#pragma unroll
  for (int c = 0; c < 3; ++c) g_copies(C2_GB + c * C2_GCH, tid + 4, ga[c], gb[c]);
  if (tid < 24) g_copies(C2_GB + ec * C2_GCH, eM, ea, eb);
#pragma unroll
  for (int c = 0; c < 3; ++c) asm volatile("" : "+v"(dsk[c]));
  KEEP(pf, uv[2][3]) KEEP(pf3, uv[1][3])
  TL_MID(1, 0)
  __syncthreads();
  TL_MID(1, 1)
  const int r = lane & 31, kg = lane >> 5;
  const u32 a0 = C2_GB + (3 - (r & 3)) * C2_CS + 2 * (2076 - 16 * 128 + 8 * kg - 4 * (r >> 2));
  const u32 b0 = C2_UB + r * 80 + kg * 16;
  const u32 y0 = r * 144 + kg * 16;
  const u32 ab = w < 3 ? a0 + w * C2_GCH + 128 * 32 : a0 + 128 * 32 - 64 * 41;
  const u32 ub = w < 3 ? b0 + w * C2_UCH + 32 * 80 : b0 + (64 - 41) * 80;
  const u32 yb = w < 3 ? y0 + w * C2_YCH : y0 + 64 * 144;
  asm volatile(
    "s_cmp_eq_u32 %[w], 3\n"
    "s_cbranch_scc1 FAR_%=\n"
    "v_subrev_u32 v60, 640, %[ab]\n"
    "v_subrev_u32 v61, 800, %[ub]\n"
    "ds_read_b64 v[112:113], v60 offset:672\n"
    "ds_read_b64 v[114:115], v60 offset:680\n"
    "ds_read_b128 v[116:119], v61 offset:832\n"
    "ds_read_b128 v[120:123], v61 offset:3392\n"
    "ds_read_b64 v[124:125], v60 offset:640\n"
    "ds_read_b64 v[126:127], v60 offset:648\n"
    "ds_read_b128 v[128:131], v61 offset:800\n"
    "ds_read_b128 v[132:135], v61 offset:3360\n"
    "ds_read_b64 v[136:137], v60 offset:608\n"
    "ds_read_b64 v[138:139], v60 offset:616\n"
    "ds_read_b128 v[140:143], v61 offset:752\n"
    "ds_read_b128 v[144:147], v61 offset:3312\n"
    "s_waitcnt lgkmcnt(8)\n"
    "v_mfma_f32_32x32x16_f16 v[64:79], v[112:115], v[116:119], 0\n"
    "ds_read_b64 v[148:149], v60 offset:576\n"
    "ds_read_b64 v[150:151], v60 offset:584\n"
    "v_mfma_f32_32x32x16_f16 v[80:95], v[112:115], v[120:123], 0\n"
    "ds_read_b128 v[152:155], v61 offset:720\n"
    "ds_read_b128 v[156:159], v61 offset:3280\n"
    "s_waitcnt lgkmcnt(8)\n"
    "v_mfma_f32_32x32x16_f16 v[64:79], v[124:127], v[128:131], v[64:79]\n"
    "ds_read_b64 v[112:113], v60 offset:544\n"
    "ds_read_b64 v[114:115], v60 offset:552\n"
    "v_mfma_f32_32x32x16_f16 v[80:95], v[124:127], v[132:135], v[80:95]\n"
    "ds_read_b128 v[116:119], v61 offset:672\n"
    "ds_read_b128 v[120:123], v61 offset:3232\n"
    "s_waitcnt lgkmcnt(8)\n"
    "v_mfma_f32_32x32x16_f16 v[64:79], v[136:139], v[140:143], v[64:79]\n"
    "ds_read_b64 v[124:125], v60 offset:512\n"
    "ds_read_b64 v[126:127], v60 offset:520\n"
    "v_mfma_f32_32x32x16_f16 v[80:95], v[136:139], v[144:147], v[80:95]\n"
    "ds_read_b128 v[128:131], v61 offset:640\n"
    "ds_read_b128 v[132:135], v61 offset:3200\n"
    "s_waitcnt lgkmcnt(8)\n"
    "v_mfma_f32_32x32x16_f16 v[64:79], v[148:151], v[152:155], v[64:79]\n"
    "ds_read_b64 v[136:137], v60 offset:480\n"
    "ds_read_b64 v[138:139], v60 offset:488\n"
    "v_mfma_f32_32x32x16_f16 v[80:95], v[148:151], v[156:159], v[80:95]\n"
    "ds_read_b128 v[140:143], v61 offset:592\n"
    "ds_read_b128 v[144:147], v61 offset:3152\n"
    "v_subrev_u32 v60, 128, v60\n"
    "v_subrev_u32 v61, 160, v61\n"
    "s_mov_b32 s40, 14\n"
    "NL_%=:\n"
    "s_waitcnt lgkmcnt(8)\n"
    "v_mfma_f32_32x32x16_f16 v[64:79], v[112:115], v[116:119], v[64:79]\n"
    "ds_read_b64 v[148:149], v60 offset:576\n"
    "ds_read_b64 v[150:151], v60 offset:584\n"
    "v_mfma_f32_32x32x16_f16 v[80:95], v[112:115], v[120:123], v[80:95]\n"
    "ds_read_b128 v[152:155], v61 offset:720\n"
    "ds_read_b128 v[156:159], v61 offset:3280\n"
    "s_waitcnt lgkmcnt(8)\n"
    "v_mfma_f32_32x32x16_f16 v[64:79], v[124:127], v[128:131], v[64:79]\n"
    "ds_read_b64 v[112:113], v60 offset:544\n"
    "ds_read_b64 v[114:115], v60 offset:552\n"
    "v_mfma_f32_32x32x16_f16 v[80:95], v[124:127], v[132:135], v[80:95]\n"
    "ds_read_b128 v[116:119], v61 offset:672\n"
    "ds_read_b128 v[120:123], v61 offset:3232\n"
    "s_waitcnt lgkmcnt(8)\n"
    "v_mfma_f32_32x32x16_f16 v[64:79], v[136:139], v[140:143], v[64:79]\n"
    "ds_read_b64 v[124:125], v60 offset:512\n"
    "ds_read_b64 v[126:127], v60 offset:520\n"
    "v_mfma_f32_32x32x16_f16 v[80:95], v[136:139], v[144:147], v[80:95]\n"
    "ds_read_b128 v[128:131], v61 offset:640\n"
    "ds_read_b128 v[132:135], v61 offset:3200\n"
    "s_waitcnt lgkmcnt(8)\n"
    "v_mfma_f32_32x32x16_f16 v[64:79], v[148:151], v[152:155], v[64:79]\n"
    "ds_read_b64 v[136:137], v60 offset:480\n"
    "ds_read_b64 v[138:139], v60 offset:488\n"
    "v_mfma_f32_32x32x16_f16 v[80:95], v[148:151], v[156:159], v[80:95]\n"
    "ds_read_b128 v[140:143], v61 offset:592\n"
    "ds_read_b128 v[144:147], v61 offset:3152\n"
    "v_subrev_u32 v60, 128, v60\n"
    "v_subrev_u32 v61, 160, v61\n"
    "s_sub_u32 s40, s40, 1\n"
    "s_cmp_lg_u32 s40, 0\n"
    "s_cbranch_scc1 NL_%=\n"
    "s_waitcnt lgkmcnt(8)\n"
    "v_mfma_f32_32x32x16_f16 v[64:79], v[112:115], v[116:119], v[64:79]\n"
    "ds_read_b64 v[148:149], v60 offset:576\n"
    "ds_read_b64 v[150:151], v60 offset:584\n"
    "v_mfma_f32_32x32x16_f16 v[80:95], v[112:115], v[120:123], v[80:95]\n"
    "ds_read_b128 v[152:155], v61 offset:720\n"
    "ds_read_b128 v[156:159], v61 offset:3280\n"
    "s_waitcnt lgkmcnt(8)\n"
    "v_mfma_f32_32x32x16_f16 v[64:79], v[124:127], v[128:131], v[64:79]\n"
    "ds_read_b64 v[112:113], v60 offset:544\n"
    "ds_read_b64 v[114:115], v60 offset:552\n"
    "v_mfma_f32_32x32x16_f16 v[80:95], v[124:127], v[132:135], v[80:95]\n"
    "ds_read_b128 v[116:119], v61 offset:672\n"
    "ds_read_b128 v[120:123], v61 offset:3232\n"
    "s_waitcnt lgkmcnt(8)\n"
    "v_mfma_f32_32x32x16_f16 v[64:79], v[136:139], v[140:143], v[64:79]\n"
    "ds_read_b64 v[124:125], v60 offset:512\n"
    "ds_read_b64 v[126:127], v60 offset:520\n"
    "v_mfma_f32_32x32x16_f16 v[80:95], v[136:139], v[144:147], v[80:95]\n"
    "ds_read_b128 v[128:131], v61 offset:640\n"
    "ds_read_b128 v[132:135], v61 offset:3200\n"
    "s_waitcnt lgkmcnt(8)\n"
    "v_mfma_f32_32x32x16_f16 v[64:79], v[148:151], v[152:155], v[64:79]\n"
    "ds_read_b64 v[136:137], v60 offset:480\n"
    "ds_read_b64 v[138:139], v60 offset:488\n"
    "v_mfma_f32_32x32x16_f16 v[80:95], v[148:151], v[156:159], v[80:95]\n"
    "ds_read_b128 v[144:147], v61 offset:3152\n"
    "s_waitcnt lgkmcnt(7)\n"
    "v_mfma_f32_32x32x16_f16 v[64:79], v[112:115], v[116:119], v[64:79]\n"
    "ds_read_b64 v[148:149], v60 offset:448\n"
    "ds_read_b64 v[150:151], v60 offset:456\n"
    "v_mfma_f32_32x32x16_f16 v[80:95], v[112:115], v[120:123], v[80:95]\n"
    "ds_read_b128 v[156:159], v61 offset:3120\n"
    "s_waitcnt lgkmcnt(6)\n"
    "v_mfma_f32_32x32x16_f16 v[64:79], v[124:127], v[128:131], v[64:79]\n"
    "ds_read_b64 v[112:113], v60 offset:416\n"
    "ds_read_b64 v[114:115], v60 offset:424\n"
    "v_mfma_f32_32x32x16_f16 v[80:95], v[124:127], v[132:135], v[80:95]\n"
    "ds_read_b128 v[120:123], v61 offset:3072\n"
    "ds_read_b64 v[124:125], v60 offset:384\n"
    "ds_read_b64 v[126:127], v60 offset:392\n"
    "s_waitcnt lgkmcnt(8)\n"
    "v_mfma_f32_32x32x16_f16 v[80:95], v[136:139], v[144:147], v[80:95]\n"
    "ds_read_b128 v[132:135], v61 offset:3040\n"
    "ds_read_b64 v[136:137], v60 offset:352\n"
    "ds_read_b64 v[138:139], v60 offset:360\n"
    "s_waitcnt lgkmcnt(8)\n"
    "v_mfma_f32_32x32x16_f16 v[96:111], v[148:151], v[156:159], 0\n"
    "ds_read_b128 v[144:147], v61 offset:2992\n"
    "ds_read_b64 v[148:149], v60 offset:320\n"
    "ds_read_b64 v[150:151], v60 offset:328\n"
    "s_waitcnt lgkmcnt(8)\n"
    "v_mfma_f32_32x32x16_f16 v[80:95], v[112:115], v[120:123], v[80:95]\n"
    "ds_read_b128 v[156:159], v61 offset:2960\n"
    "ds_read_b64 v[112:113], v60 offset:288\n"
    "ds_read_b64 v[114:115], v60 offset:296\n"
    "s_waitcnt lgkmcnt(8)\n"
    "v_mfma_f32_32x32x16_f16 v[96:111], v[124:127], v[132:135], v[96:111]\n"
    "ds_read_b128 v[120:123], v61 offset:2912\n"
    "ds_read_b64 v[124:125], v60 offset:256\n"
    "ds_read_b64 v[126:127], v60 offset:264\n"
    "s_waitcnt lgkmcnt(8)\n"
    "v_mfma_f32_32x32x16_f16 v[80:95], v[136:139], v[144:147], v[80:95]\n"
    "ds_read_b128 v[132:135], v61 offset:2880\n"
    "ds_read_b64 v[136:137], v60 offset:224\n"
    "ds_read_b64 v[138:139], v60 offset:232\n"
    "s_waitcnt lgkmcnt(8)\n"
    "v_mfma_f32_32x32x16_f16 v[96:111], v[148:151], v[156:159], v[96:111]\n"
    "ds_read_b128 v[144:147], v61 offset:2832\n"
    "ds_read_b64 v[148:149], v60 offset:192\n"
    "ds_read_b64 v[150:151], v60 offset:200\n"
    "s_waitcnt lgkmcnt(8)\n"
    "v_mfma_f32_32x32x16_f16 v[80:95], v[112:115], v[120:123], v[80:95]\n"
    "ds_read_b128 v[156:159], v61 offset:2800\n"
    "ds_read_b64 v[112:113], v60 offset:160\n"
    "ds_read_b64 v[114:115], v60 offset:168\n"
    "s_waitcnt lgkmcnt(8)\n"
    "v_mfma_f32_32x32x16_f16 v[96:111], v[124:127], v[132:135], v[96:111]\n"
    "ds_read_b128 v[120:123], v61 offset:2752\n"
    "ds_read_b64 v[124:125], v60 offset:128\n"
    "ds_read_b64 v[126:127], v60 offset:136\n"
    "s_waitcnt lgkmcnt(8)\n"
    "v_mfma_f32_32x32x16_f16 v[80:95], v[136:139], v[144:147], v[80:95]\n"
    "ds_read_b128 v[132:135], v61 offset:2720\n"
    "ds_read_b64 v[136:137], v60 offset:96\n"
    "ds_read_b64 v[138:139], v60 offset:104\n"
    "s_waitcnt lgkmcnt(8)\n"
    "v_mfma_f32_32x32x16_f16 v[96:111], v[148:151], v[156:159], v[96:111]\n"
    "ds_read_b128 v[144:147], v61 offset:2672\n"
    "ds_read_b64 v[148:149], v60 offset:64\n"
    "ds_read_b64 v[150:151], v60 offset:72\n"
    "s_waitcnt lgkmcnt(8)\n"
    "v_mfma_f32_32x32x16_f16 v[80:95], v[112:115], v[120:123], v[80:95]\n"
    "ds_read_b128 v[156:159], v61 offset:2640\n"
    "ds_read_b64 v[112:113], v60 offset:32\n"
    "ds_read_b64 v[114:115], v60 offset:40\n"
    "s_waitcnt lgkmcnt(8)\n"
    "v_mfma_f32_32x32x16_f16 v[96:111], v[124:127], v[132:135], v[96:111]\n"
    "ds_read_b128 v[120:123], v61 offset:2592\n"
    "ds_read_b64 v[124:125], v60 offset:0\n"
    "ds_read_b64 v[126:127], v60 offset:8\n"
    "s_waitcnt lgkmcnt(8)\n"
    "v_mfma_f32_32x32x16_f16 v[80:95], v[136:139], v[144:147], v[80:95]\n"
    "ds_read_b128 v[132:135], v61 offset:2560\n"
    "s_waitcnt lgkmcnt(6)\n"
    "v_mfma_f32_32x32x16_f16 v[96:111], v[148:151], v[156:159], v[96:111]\n"
    "s_waitcnt lgkmcnt(3)\n"
    "v_mfma_f32_32x32x16_f16 v[80:95], v[112:115], v[120:123], v[80:95]\n"
    "s_waitcnt lgkmcnt(0)\n"
    "v_mfma_f32_32x32x16_f16 v[96:111], v[124:127], v[132:135], v[96:111]\n"
    "s_nop 7\n"
    "s_nop 7\n"
    "v_add_f32 v80, v80, v96\n"
    "v_add_f32 v81, v81, v97\n"
    "v_add_f32 v82, v82, v98\n"
    "v_add_f32 v83, v83, v99\n"
    "v_add_f32 v84, v84, v100\n"
    "v_add_f32 v85, v85, v101\n"
    "v_add_f32 v86, v86, v102\n"
    "v_add_f32 v87, v87, v103\n"
    "v_add_f32 v88, v88, v104\n"
    "v_add_f32 v89, v89, v105\n"
    "v_add_f32 v90, v90, v106\n"
    "v_add_f32 v91, v91, v107\n"
    "v_add_f32 v92, v92, v108\n"
    "v_add_f32 v93, v93, v109\n"
    "v_add_f32 v94, v94, v110\n"
    "v_add_f32 v95, v95, v111\n"
    "s_barrier\n"
    "ds_write_b128 %[y], v[64:67] offset:0\n"
    "ds_write_b128 %[y], v[68:71] offset:32\n"
    "ds_write_b128 %[y], v[72:75] offset:64\n"
    "ds_write_b128 %[y], v[76:79] offset:96\n"
    "ds_write_b128 %[y], v[80:83] offset:4608\n"
    "ds_write_b128 %[y], v[84:87] offset:4640\n"
    "ds_write_b128 %[y], v[88:91] offset:4672\n"
    "ds_write_b128 %[y], v[92:95] offset:4704\n"
    "s_branch END_%=\n"
    "FAR_%=:\n"
    "v_subrev_u32 v60, 128, %[ab]\n"
    "v_subrev_u32 v61, 800, %[ub]\n"
    "ds_read_b64 v[112:113], v60 offset:160\n"
    "ds_read_b64 v[114:115], v60 offset:168\n"
    "ds_read_b128 v[116:119], v61 offset:832\n"
    "ds_read_b64 v[120:121], v60 offset:17824\n"
    "ds_read_b64 v[122:123], v60 offset:17832\n"
    "ds_read_b128 v[124:127], v61 offset:8512\n"
    "ds_read_b64 v[128:129], v60 offset:35488\n"
    "ds_read_b64 v[130:131], v60 offset:35496\n"
    "ds_read_b128 v[132:135], v61 offset:16192\n"
    "ds_read_b64 v[136:137], v60 offset:128\n"
    "ds_read_b64 v[138:139], v60 offset:136\n"
    "ds_read_b128 v[140:143], v61 offset:800\n"
    "s_waitcnt lgkmcnt(9)\n"
    "v_mfma_f32_32x32x16_f16 v[64:79], v[112:115], v[116:119], 0\n"
    "ds_read_b64 v[144:145], v60 offset:17792\n"
    "ds_read_b64 v[146:147], v60 offset:17800\n"
    "ds_read_b128 v[148:151], v61 offset:8480\n"
    "s_waitcnt lgkmcnt(9)\n"
    "v_mfma_f32_32x32x16_f16 v[80:95], v[120:123], v[124:127], 0\n"
    "ds_read_b64 v[152:153], v60 offset:35456\n"
    "ds_read_b64 v[154:155], v60 offset:35464\n"
    "ds_read_b128 v[156:159], v61 offset:16160\n"
    "s_waitcnt lgkmcnt(9)\n"
    "v_mfma_f32_32x32x16_f16 v[96:111], v[128:131], v[132:135], 0\n"
    "ds_read_b64 v[112:113], v60 offset:96\n"
    "ds_read_b64 v[114:115], v60 offset:104\n"
    "ds_read_b128 v[116:119], v61 offset:752\n"
    "s_waitcnt lgkmcnt(9)\n"
    "v_mfma_f32_32x32x16_f16 v[64:79], v[136:139], v[140:143], v[64:79]\n"
    "ds_read_b64 v[120:121], v60 offset:17760\n"
    "ds_read_b64 v[122:123], v60 offset:17768\n"
    "ds_read_b128 v[124:127], v61 offset:8432\n"
    "s_waitcnt lgkmcnt(9)\n"
    "v_mfma_f32_32x32x16_f16 v[80:95], v[144:147], v[148:151], v[80:95]\n"
    "ds_read_b64 v[128:129], v60 offset:35424\n"
    "ds_read_b64 v[130:131], v60 offset:35432\n"
    "ds_read_b128 v[132:135], v61 offset:16112\n"
    "s_waitcnt lgkmcnt(9)\n"
    "v_mfma_f32_32x32x16_f16 v[96:111], v[152:155], v[156:159], v[96:111]\n"
    "ds_read_b64 v[136:137], v60 offset:64\n"
    "ds_read_b64 v[138:139], v60 offset:72\n"
    "ds_read_b128 v[140:143], v61 offset:720\n"
    "v_subrev_u32 v60, 64, v60\n"
    "v_subrev_u32 v61, 80, v61\n"
    "s_mov_b32 s40, 22\n"
    "FL_%=:\n"
    "s_waitcnt lgkmcnt(9)\n"
    "v_mfma_f32_32x32x16_f16 v[64:79], v[112:115], v[116:119], v[64:79]\n"
    "ds_read_b64 v[144:145], v60 offset:17792\n"
    "ds_read_b64 v[146:147], v60 offset:17800\n"
    "ds_read_b128 v[148:151], v61 offset:8480\n"
    "s_waitcnt lgkmcnt(9)\n"
    "v_mfma_f32_32x32x16_f16 v[80:95], v[120:123], v[124:127], v[80:95]\n"
    "ds_read_b64 v[152:153], v60 offset:35456\n"
    "ds_read_b64 v[154:155], v60 offset:35464\n"
    "ds_read_b128 v[156:159], v61 offset:16160\n"
    "s_waitcnt lgkmcnt(9)\n"
    "v_mfma_f32_32x32x16_f16 v[96:111], v[128:131], v[132:135], v[96:111]\n"
    "ds_read_b64 v[112:113], v60 offset:96\n"
    "ds_read_b64 v[114:115], v60 offset:104\n"
    "ds_read_b128 v[116:119], v61 offset:752\n"
    "s_waitcnt lgkmcnt(9)\n"
    "v_mfma_f32_32x32x16_f16 v[64:79], v[136:139], v[140:143], v[64:79]\n"
    "ds_read_b64 v[120:121], v60 offset:17760\n"
    "ds_read_b64 v[122:123], v60 offset:17768\n"
    "ds_read_b128 v[124:127], v61 offset:8432\n"
    "s_waitcnt lgkmcnt(9)\n"
    "v_mfma_f32_32x32x16_f16 v[80:95], v[144:147], v[148:151], v[80:95]\n"
    "ds_read_b64 v[128:129], v60 offset:35424\n"
    "ds_read_b64 v[130:131], v60 offset:35432\n"
    "ds_read_b128 v[132:135], v61 offset:16112\n"
    "s_waitcnt lgkmcnt(9)\n"
    "v_mfma_f32_32x32x16_f16 v[96:111], v[152:155], v[156:159], v[96:111]\n"
    "ds_read_b64 v[136:137], v60 offset:64\n"
    "ds_read_b64 v[138:139], v60 offset:72\n"
    "ds_read_b128 v[140:143], v61 offset:720\n"
    "v_subrev_u32 v60, 64, v60\n"
    "v_subrev_u32 v61, 80, v61\n"
    "s_sub_u32 s40, s40, 1\n"
    "s_cmp_lg_u32 s40, 0\n"
    "s_cbranch_scc1 FL_%=\n"
    "s_waitcnt lgkmcnt(9)\n"
    "v_mfma_f32_32x32x16_f16 v[64:79], v[112:115], v[116:119], v[64:79]\n"
    "ds_read_b64 v[144:145], v60 offset:17792\n"
    "ds_read_b64 v[146:147], v60 offset:17800\n"
    "ds_read_b128 v[148:151], v61 offset:8480\n"
    "s_waitcnt lgkmcnt(9)\n"
    "v_mfma_f32_32x32x16_f16 v[80:95], v[120:123], v[124:127], v[80:95]\n"
    "ds_read_b64 v[152:153], v60 offset:35456\n"
    "ds_read_b64 v[154:155], v60 offset:35464\n"
    "ds_read_b128 v[156:159], v61 offset:16160\n"
    "s_waitcnt lgkmcnt(9)\n"
    "v_mfma_f32_32x32x16_f16 v[96:111], v[128:131], v[132:135], v[96:111]\n"
    "s_waitcnt lgkmcnt(6)\n"
    "v_mfma_f32_32x32x16_f16 v[64:79], v[136:139], v[140:143], v[64:79]\n"
    "s_waitcnt lgkmcnt(3)\n"
    "v_mfma_f32_32x32x16_f16 v[80:95], v[144:147], v[148:151], v[80:95]\n"
    "s_waitcnt lgkmcnt(0)\n"
    "v_mfma_f32_32x32x16_f16 v[96:111], v[152:155], v[156:159], v[96:111]\n"
    "s_nop 7\n"
    "s_nop 7\n"
    "s_barrier\n"
    "ds_write_b128 %[y], v[64:67] offset:0\n"
    "ds_write_b128 %[y], v[68:71] offset:32\n"
    "ds_write_b128 %[y], v[72:75] offset:64\n"
    "ds_write_b128 %[y], v[76:79] offset:96\n"
    "ds_write_b128 %[y], v[80:83] offset:13824\n"
    "ds_write_b128 %[y], v[84:87] offset:13856\n"
    "ds_write_b128 %[y], v[88:91] offset:13888\n"
    "ds_write_b128 %[y], v[92:95] offset:13920\n"
    "ds_write_b128 %[y], v[96:99] offset:27648\n"
    "ds_write_b128 %[y], v[100:103] offset:27680\n"
    "ds_write_b128 %[y], v[104:107] offset:27712\n"
    "ds_write_b128 %[y], v[108:111] offset:27744\n"
    "END_%=:\n"
    "s_waitcnt lgkmcnt(0)\n"
    :
    : [ab] "v"(ab), [ub] "v"(ub), [y] "v"(yb), [w] "s"(w)
    : "v60", "v61", "v62", "v63", "v64", "v65", "v66", "v67", "v68", "v69", "v70", "v71", "v72", "v73", "v74", "v75", "v76", "v77", "v78", "v79", "v80", "v81", "v82", "v83", "v84", "v85", "v86", "v87", "v88", "v89", "v90", "v91", "v92", "v93", "v94", "v95", "v96", "v97", "v98", "v99", "v100", "v101", "v102", "v103", "v104", "v105", "v106", "v107", "v108", "v109", "v110", "v111", "v112", "v113", "v114", "v115", "v116", "v117", "v118", "v119", "v120", "v121", "v122", "v123", "v124", "v125", "v126", "v127", "v128", "v129", "v130", "v131", "v132", "v133", "v134", "v135", "v136", "v137", "v138", "v139", "v140", "v141", "v142", "v143", "v144", "v145", "v146", "v147", "v148", "v149", "v150", "v151", "v152", "v153", "v154", "v155", "v156", "v157", "v158", "v159", "s40", "scc", "memory"
  );
  TL_MID(1, 3)
  __syncthreads();
#pragma unroll
  for (int c = 0; c < 3; ++c) {
    const u32 ya = c * C2_YCH + (tid >> 2) * 144 + (tid & 3) * 32;
    f32x4 lo = lds_ld<f32x4>(ya), hi = lds_ld<f32x4>(ya + 16);
    if (w >= 2) { lo += lds_ld<f32x4>(ya + 32 * 144); hi += lds_ld<f32x4>(ya + 32 * 144 + 16); }
    u32x4 o;
#pragma unroll
    for (int k = 0; k < 4; ++k) {
      const float ya0 = (k < 2 ? lo[2 * k] : hi[2 * k - 4]) * isc[c], ya1 = (k < 2 ? lo[2 * k + 1] : hi[2 * k - 3]) * isc[c];
      const u32 yb = cvt_pk_bf16(ya0, ya1);
      const u32 sk = cvt_pk_bf16(bf_lo(uv[c][k]) * dsk[c], bf_hi(uv[c][k]) * dsk[c]);
      const u32 aa = cvt_pk_bf16(bf_lo(yb) + bf_lo(sk), bf_hi(yb) + bf_hi(sk));
      o[k] = cvt_pk_bf16(bf_lo(xv[c][k]) + bf_lo(aa), bf_hi(xv[c][k]) + bf_hi(aa));
    }
    st16_wt(ws + WS_HT + (size_t)(d0 + c) * T_ROW + 16 * tid, o);
  }
  TL_MID(1, 4)
  LABEL("K2_CODE_E")
  TL_END(1)
}

extern "C" void kernel_launch(void* const* d_in, const int* in_sizes, int n_in, void* d_out, int out_size, void* d_ws,
                              size_t ws_size, hipStream_t stream) {
  char* ws = (char*)d_ws;
  (void)hipFuncSetAttribute((const void*)k1_pre, hipFuncAttributeMaxDynamicSharedMemorySize, K1_LDS);
  (void)hipFuncSetAttribute((const void*)k2_conv, hipFuncAttributeMaxDynamicSharedMemorySize, K2_LDS);
  (void)hipFuncSetAttribute((const void*)k3_mlp, hipFuncAttributeMaxDynamicSharedMemorySize, K3_LDS);
  k1_pre<<<K1_GRID, 256, K1_LDS, stream>>>((const u16*)d_in[0], (const u16*)d_in[1], (const u16*)d_in[2], (const u16*)d_in[3],
                                           (const u16*)d_in[4], (const u16*)d_in[5], (const u16*)d_in[8], (const u16*)d_in[10], ws);
  k2_conv<<<K2_GRID, 256, K2_LDS, stream>>>((const u16*)d_in[6], (const u16*)d_in[8], (const u16*)d_in[10], ws, ws);
  k3_mlp<<<256, 256, K3_LDS, stream>>>(ws, ws, (const u16*)d_in[9], (const u16*)d_in[11], (u16*)d_out);
}
```
